```python
import torch

def _softmax(x, axis=-1):
    return torch.softmax(x, dim=axis)

def _gelu(x):
    return torch.nn.functional.gelu(x, approximate='tanh')

def _silu(x):
    return torch.nn.functional.silu(x)

def _sigmoid(x):
    return torch.sigmoid(x)

def _layernorm(x, axis=-1, eps=1e-05):
    m = x.mean(dim=axis, keepdim=True)
    v = x.var(dim=axis, keepdim=True, unbiased=False)
    return (x - m) / torch.sqrt(v + eps)

def _scatter_add(tgt, idx, src, axis=0):
    return tgt.clone().index_add_(axis, idx, src)

def _segment_sum(src, seg, n):
    out = torch.zeros((n,) + src.shape[1:], dtype=src.dtype)
    return out.index_add_(0, seg, src)

def _pad(x, pw):
    flat = tuple((v for pair in reversed(pw) for v in pair))
    return torch.nn.functional.pad(x, flat)
kernel_name = 'standalone__tree_speculative_verify_tree_attention_over_candidate_token_tree__sampling'

def setup_inputs():

    _w = lambda a: (lambda t: t.to(torch.bfloat16) if t.is_floating_point() else t)(torch.as_tensor(a))
    B, Hq, Hkv, T, C, D = (8, 32, 4, 32, 8192, 128)
    tree_mask = torch.zeros((B, T, T), dtype=float)
    for b in range(B):
        parent = torch.zeros(T, dtype=torch.int64)
        for i in range(1, T):
            parent[i] = int(torch.randint(0, i, (1,)).item())
        for i in range(T):
            j = i
            tree_mask[b, i, j] = 1.0
            while j > 0:
                j = parent[j]
                tree_mask[b, i, j] = 1.0
    return {'q': _w((lambda *s: torch.randn(*s, dtype=torch.bfloat16) * 0.8)(B, Hq, T, D)), 'k_cache': _w((lambda *s: torch.randn(*s, dtype=torch.bfloat16) * 0.8)(B, Hkv, C, D)), 'v_cache': _w((lambda *s: torch.randn(*s, dtype=torch.bfloat16) * 0.05)(B, Hkv, C, D)), 'k_tree': _w((lambda *s: torch.randn(*s, dtype=torch.bfloat16) * 0.8)(B, Hkv, T, D)), 'v_tree': _w((lambda *s: torch.randn(*s, dtype=torch.bfloat16) * 0.05)(B, Hkv, T, D)), 'tree_mask': _w(tree_mask)}

def reference(q, k_cache, v_cache, k_tree, v_tree, tree_mask):
    B, Hq, T, D = q.shape
    Hkv, C = (k_cache.shape[1], k_cache.shape[2])
    rep = Hq // Hkv
    kc = (lambda x, n, axis: torch.repeat_interleave(x, n, dim=axis))(k_cache, rep, axis=1)
    vc = (lambda x, n, axis: torch.repeat_interleave(x, n, dim=axis))(v_cache, rep, axis=1)
    kt = (lambda x, n, axis: torch.repeat_interleave(x, n, dim=axis))(k_tree, rep, axis=1)
    vt = (lambda x, n, axis: torch.repeat_interleave(x, n, dim=axis))(v_tree, rep, axis=1)
    k = torch.concatenate([kc, kt], axis=2)
    v = torch.concatenate([vc, vt], axis=2)
    scale = D ** (-0.5)
    scores = torch.einsum('bhqd,bhkd->bhqk', q, k) * scale
    ctx_mask = torch.ones((B, 1, T, C), dtype=scores.dtype)
    tm = tree_mask[:, None, :, :].clone()
    full_mask = torch.concatenate([ctx_mask, tm], axis=-1)
    scores = scores + (1.0 - full_mask) * -1000000000.0
    p = _softmax(scores, axis=-1)
    return torch.einsum('bhqk,bhkd->bhqd', p, v)
```

```cpp
#include <hip/hip_runtime.h>
#include <hip/hip_bf16.h>
#include <cstdio>

#define ATTN_B 8
#define ATTN_HQ 32
#define ATTN_HKV 4
#define ATTN_SQ 32
#define ATTN_SK 8192
#define ATTN_NSPLIT 8
#define ATTN_THR 8
#define ATTN_KVCONV 0
#define ATTN_CAUSAL 0
#define ATTN_LENS 0
#define ATTN_QPARK (4 + ATTN_LENS)
#define ATTN_WG4 1
#define ATTN_QPARK4 4
#define ATTN_XCHG4 1
constexpr int B = ATTN_B, HQ = ATTN_HQ, HKV = ATTN_HKV, SQ = ATTN_SQ, SK = ATTN_SK;
constexpr int NSPLIT = ATTN_NSPLIT;
constexpr float THR = ATTN_THR;
constexpr int CAUSAL = ATTN_CAUSAL;
constexpr int HAS_LENS = ATTN_LENS;
constexpr int KV_CONVERT = ATTN_KVCONV;
constexpr int QPARK = ATTN_QPARK;
constexpr int WG4 = ATTN_WG4;
constexpr int XCHG4 = ATTN_XCHG4;
constexpr int QPARK4 = ATTN_QPARK4;
constexpr int D = 128, NW = 8, QBLK = 32, KVBLK = 64;
constexpr float SCALE = 0.088388347648318447f;
constexpr int G = HQ / HKV, ROWS = G * SQ, NT = SK / KVBLK, TPS = NT / NSPLIT;
constexpr int NWB = (WG4 && ROWS <= QBLK * 4 && !CAUSAL && !HAS_LENS && TPS % 4 == 0) ? 4 : NW, NSB = NSPLIT * (NW / NWB), TPSB = NT / NSB;
static_assert(HQ % HKV == 0 && ROWS >= 1 && ROWS <= QBLK * NW, "G*SQ must fit the workgroup's 256 MFMA rows (larger SQ -> dense body)");
static_assert(!(CAUSAL && HAS_LENS && !KV_CONVERT), "CAUSAL + HAS_LENS on the f32 convert-on-load loop spills 2 VGPRs (ROCm 7.2, QPARK 4..8): set KV_CONVERT 1 (bf16 loop, 0 spill) for that combination");
static_assert(SK % KVBLK == 0 && NT % NSPLIT == 0 && TPS >= 2 && TPS % 2 == 0, "SK must give every split an even number (>= 2) of 64-key tiles");
static_assert(NT % NSB == 0 && TPSB >= 2 && TPSB % 2 == 0, "internal: half-split tile count (NWB falls back to 8 waves when TPS % 4 != 0)");
constexpr float L2E = 1.4426950408889634f, SL2E = SCALE * L2E, THR2 = THR * L2E;
constexpr size_t SHM_V = KVBLK * D * 2, SHM_K = KVBLK * D * 2, SHM_WS = 2 * SHM_V + 2 * SHM_K;
template <int NWT> constexpr size_t shm_qp() { return SHM_WS + ((NWT == NW || !XCHG4) ? (size_t)NWT * 64 * 4 : 0); }
static_assert(QPARK >= 0 && QPARK <= 8 && QPARK4 >= 0 && QPARK4 <= 8, "QPARK");
constexpr size_t WS_PML = 4096, PML_BYTES = (size_t)B * HQ * SQ * NSB * 2 * 4;
constexpr size_t WS_PO = (WS_PML + PML_BYTES + 255) / 256 * 256, WS_PEND = NSB > 1 ? WS_PO + (size_t)B * HQ * SQ * NSB * D * 4 : WS_PML;
constexpr size_t NQ = (size_t)B * HQ * SQ * D, NKV = (size_t)B * HKV * SK * D;
constexpr size_t WS_QB = (WS_PEND + 255) / 256 * 256, WS_KB = WS_QB + NQ * 2, WS_VB = WS_KB + NKV * 2;
constexpr size_t WS_END = KV_CONVERT ? WS_VB + NKV * 2 : WS_PEND;

using bf16 = __hip_bfloat16;
using bf16x8 = __attribute__((ext_vector_type(8))) short;
using s16x4 = __attribute__((ext_vector_type(4))) short;
using f32x16 = __attribute__((ext_vector_type(16))) float;
using f32x4 = __attribute__((ext_vector_type(4))) float;
using u32x4 = __attribute__((ext_vector_type(4))) unsigned;
template <class T> struct InCfg;
template <> struct InCfg<__hip_bfloat16> { static constexpr int NWT = NWB, QP = NWB == 4 ? QPARK4 : 0; };
template <> struct InCfg<float> { static constexpr int NWT = NW, QP = QPARK; };
template <class T> constexpr int nw_for() { return InCfg<T>::NWT; }
template <class T> constexpr int ns_for() { return NSPLIT * (NW / InCfg<T>::NWT); }
template <class T> constexpr size_t shm_bytes() { return shm_qp<InCfg<T>::NWT>() + (size_t)InCfg<T>::QP * 64 * InCfg<T>::NWT * 16; }
#define KSWZ(row, colB) ((row) * 256 + ((colB) ^ (((row) & 7) << 4)))
#define SBAR() __builtin_amdgcn_sched_barrier(0)
__device__ __forceinline__ int crow(int r, int hi) { return (r & 3) + 8 * (r >> 2) + 4 * hi; }
__device__ __forceinline__ unsigned cvtpk(float lo, float hi) { unsigned r; asm volatile("v_cvt_pk_bf16_f32 %0, %1, %2" : "=v"(r) : "v"(lo), "v"(hi)); return r; }
__device__ __forceinline__ bf16x8 pack8(f32x4 a, f32x4 b) { u32x4 w = {cvtpk(a[0], a[1]), cvtpk(a[2], a[3]), cvtpk(b[0], b[1]), cvtpk(b[2], b[3])}; return *reinterpret_cast<bf16x8*>(&w); }
__device__ __forceinline__ bf16x8 ld8(const bf16* p) { return *reinterpret_cast<const bf16x8*>(p); }
__device__ __forceinline__ bf16x8 ld8(const float* p) { return pack8(*reinterpret_cast<const f32x4*>(p), *reinterpret_cast<const f32x4*>(p + 4)); }
template <class T, int NR> struct Stage;
template <> struct Stage<bf16, 2> { bf16x8 v0, v1, k0, k1;
  __device__ __forceinline__ void load(const bf16* V0, const bf16* V1, const bf16* K0, const bf16* K1) { v0 = ld8(V0); v1 = ld8(V1); k0 = ld8(K0); k1 = ld8(K1); }
  __device__ __forceinline__ bf16x8 gv0() const { return v0; } __device__ __forceinline__ bf16x8 gv1() const { return v1; }
  __device__ __forceinline__ bf16x8 gk0() const { return k0; } __device__ __forceinline__ bf16x8 gk1() const { return k1; } };
template <> struct Stage<float, 2> { f32x4 v0a, v0b, v1a, v1b, k0a, k0b, k1a, k1b;
  __device__ __forceinline__ void load(const float* V0, const float* V1, const float* K0, const float* K1) {
    v0a = *(const f32x4*)V0; v0b = *(const f32x4*)(V0 + 4); v1a = *(const f32x4*)V1; v1b = *(const f32x4*)(V1 + 4);
    k0a = *(const f32x4*)K0; k0b = *(const f32x4*)(K0 + 4); k1a = *(const f32x4*)K1; k1b = *(const f32x4*)(K1 + 4); }
  __device__ __forceinline__ bf16x8 gv0() const { return pack8(v0a, v0b); } __device__ __forceinline__ bf16x8 gv1() const { return pack8(v1a, v1b); }
  __device__ __forceinline__ bf16x8 gk0() const { return pack8(k0a, k0b); } __device__ __forceinline__ bf16x8 gk1() const { return pack8(k1a, k1b); } };
template <int NR> struct Stage<bf16, NR> { bf16x8 v[NR], k[NR];
  __device__ __forceinline__ void ldv(int i, const bf16* p) { v[i] = ld8(p); } __device__ __forceinline__ void ldk(int i, const bf16* p) { k[i] = ld8(p); }
  __device__ __forceinline__ bf16x8 gv(int i) const { return v[i]; } __device__ __forceinline__ bf16x8 gk(int i) const { return k[i]; } };
template <int NR> struct Stage<float, NR> { f32x4 va[NR], vb[NR], ka[NR], kb[NR];
  __device__ __forceinline__ void ldv(int i, const float* p) { va[i] = *(const f32x4*)p; vb[i] = *(const f32x4*)(p + 4); }
  __device__ __forceinline__ void ldk(int i, const float* p) { ka[i] = *(const f32x4*)p; kb[i] = *(const f32x4*)(p + 4); }
  __device__ __forceinline__ bf16x8 gv(int i) const { return pack8(va[i], vb[i]); } __device__ __forceinline__ bf16x8 gk(int i) const { return pack8(ka[i], kb[i]); } };
__device__ __forceinline__ int voff(int row, int sc) { return (row >> 5) * 8192 + 2 * ((sc >> 4) * 512 + (row & 31) * 16 + (sc & 8)); }

__device__ __forceinline__ void partialSM(f32x16& p0, f32x16& p1, float& m_reg, float& msub, float& alpha, int kbase, int lim, int lim0) {
  if constexpr (CAUSAL || HAS_LENS) { if (kbase + KVBLK - 1 > lim0) {
    const float ninf = -__builtin_inff(); const int hi = (threadIdx.x & 63) >> 5;
    for (int r = 0; r < 16; ++r) { const int k = kbase + crow(r, hi); p0[r] = k > lim ? ninf : p0[r]; p1[r] = k + 32 > lim ? ninf : p1[r]; } } }
  float pmax = p0[0]; for (int r = 1; r < 16; ++r) pmax = fmaxf(pmax, p0[r]); for (int r = 0; r < 16; ++r) pmax = fmaxf(pmax, p1[r]);
  { auto rr = __builtin_amdgcn_permlane32_swap(__float_as_uint(pmax), __float_as_uint(pmax), false, false);
    pmax = fmaxf(__uint_as_float(rr[0]), __uint_as_float(rr[1])); }
  pmax *= SL2E;
  if (__builtin_expect(__all(pmax - m_reg <= THR2), 1)) { alpha = 1.f; }
  else { float mn = fmaxf(m_reg, pmax); msub = (mn == -__builtin_inff()) ? 0.f : mn; alpha = __builtin_amdgcn_exp2f(m_reg - msub); m_reg = mn; }
  float nm = -msub;
  for (int r = 0; r < 16; ++r) p0[r] = fmaf(p0[r], SL2E, nm); for (int r = 0; r < 16; ++r) p1[r] = fmaf(p1[r], SL2E, nm);
  for (int r = 0; r < 16; ++r) p0[r] = __builtin_amdgcn_exp2f(p0[r]);
}
__device__ __forceinline__ void finishSM(f32x16& p0, f32x16& p1, float alpha, float& l_reg, bf16x8& pa0, bf16x8& pa1, bf16x8& pa2, bf16x8& pa3) {
  for (int r = 0; r < 16; ++r) p1[r] = __builtin_amdgcn_exp2f(p1[r]);
  float ps = 0; for (int r = 0; r < 16; ++r) ps += p0[r]; for (int r = 0; r < 16; ++r) ps += p1[r];
  { auto rr = __builtin_amdgcn_permlane32_swap(__float_as_uint(ps), __float_as_uint(ps), false, false); ps = __uint_as_float(rr[0]) + __uint_as_float(rr[1]); }
  l_reg = l_reg * alpha + ps;
#define PK4(P, BASE, OUT) do { unsigned a0 = cvtpk(P[BASE + 0], P[BASE + 1]), a1 = cvtpk(P[BASE + 2], P[BASE + 3]); \
    unsigned b0 = cvtpk(P[BASE + 4], P[BASE + 5]), b1 = cvtpk(P[BASE + 6], P[BASE + 7]); \
    auto r0 = __builtin_amdgcn_permlane32_swap(a0, b0, false, false); auto r1 = __builtin_amdgcn_permlane32_swap(a1, b1, false, false); \
    u32x4 w = {r0[0], r1[0], r0[1], r1[1]}; OUT = *reinterpret_cast<bf16x8*>(&w); } while (0)
  PK4(p0, 0, pa0); PK4(p0, 8, pa1); PK4(p1, 0, pa2); PK4(p1, 8, pa3);
#undef PK4
}
template <int QP, int NWT> __device__ __forceinline__ void qkt(f32x16& p0, f32x16& p1, const bf16* Ks, const bf16x8* qr, const char* qp, int r32, int hi) {
  p0 = f32x16{}; p1 = f32x16{};
#pragma unroll
  for (int d0 = 0; d0 < 8; ++d0) { int cb = (d0 * 16 + hi * 8) * 2;
    bf16x8 b0 = *reinterpret_cast<const bf16x8*>((const char*)Ks + KSWZ(r32, cb));
    bf16x8 b1 = *reinterpret_cast<const bf16x8*>((const char*)Ks + KSWZ(32 + r32, cb));
    const bf16x8 q = d0 < 8 - QP ? qr[d0] : *reinterpret_cast<const bf16x8*>(qp + (d0 - (8 - QP)) * (64 * NWT * 16));
    p0 = __builtin_amdgcn_mfma_f32_32x32x16_bf16(b0, q, p0, 0, 0, 0);
    p1 = __builtin_amdgcn_mfma_f32_32x32x16_bf16(b1, q, p1, 0, 0, 0); }
}
__device__ __forceinline__ void pv_d0(f32x16* o, int vb, bf16x8 pa0, bf16x8 pa1, bf16x8 pa2, bf16x8 pa3) {
#pragma unroll
  for (int d0 = 0; d0 < 4; ++d0) { s16x4 lo[4], hi[4];
#pragma unroll
    for (int ks = 0; ks < 4; ++ks) { int a = vb + d0 * 2048 + (ks >> 1) * 8192 + (ks & 1) * 512;
      asm volatile("ds_read_b64_tr_b16 %0,%1" : "=&v"(lo[ks]) : "v"(a) : "memory");
      asm volatile("ds_read_b64_tr_b16 %0,%1 offset:128" : "=&v"(hi[ks]) : "v"(a) : "memory"); }
    asm volatile("s_waitcnt lgkmcnt(0)" ::: "memory"); SBAR();
#define PK(k) (bf16x8){lo[k][0], lo[k][1], lo[k][2], lo[k][3], hi[k][0], hi[k][1], hi[k][2], hi[k][3]}
    o[d0] = __builtin_amdgcn_mfma_f32_32x32x16_bf16(pa0, PK(0), o[d0], 0, 0, 0);
    o[d0] = __builtin_amdgcn_mfma_f32_32x32x16_bf16(pa1, PK(1), o[d0], 0, 0, 0);
    o[d0] = __builtin_amdgcn_mfma_f32_32x32x16_bf16(pa2, PK(2), o[d0], 0, 0, 0);
    o[d0] = __builtin_amdgcn_mfma_f32_32x32x16_bf16(pa3, PK(3), o[d0], 0, 0, 0);
#undef PK
  }
}

template <class TQ, class TKV>
__global__ __launch_bounds__(64 * InCfg<TKV>::NWT, NW / InCfg<TKV>::NWT)
void gqa_chunk_fwd(const TQ* __restrict__ Q, const TKV* __restrict__ K, const TKV* __restrict__ V, const int* __restrict__ lens, float* __restrict__ O, float* __restrict__ pml, float* __restrict__ po) {
  constexpr int NWT = InCfg<TKV>::NWT, NS = NSPLIT * (NW / NWT), TPS = NT / NS;
  constexpr int NR = KVBLK / (4 * NWT);
  static_assert(ROWS <= QBLK * NWT, "rows must fit this form's waves");
  const int tid = threadIdx.x, wid = tid >> 6, lane = tid & 63, r32 = lane & 31, hi = lane >> 5;
  const int split = blockIdx.x, kvh = blockIdx.y, b = blockIdx.z;
  const TQ* Qg = Q + ((size_t)b * HQ + (size_t)kvh * G) * SQ * D;
  const TKV* Kh = K + (((size_t)b * HKV + kvh) * SK + (size_t)split * TPS * KVBLK) * D;
  const TKV* Vh = V + (((size_t)b * HKV + kvh) * SK + (size_t)split * TPS * KVBLK) * D;
  extern __shared__ __attribute__((aligned(16))) char shm[];
  bf16* V_lds = (bf16*)shm; bf16* K_lds = (bf16*)(shm + 2 * SHM_V);
  float* ws = (float*)(shm + SHM_WS) + wid * 64; float* li_l = ws; float* al_l = ws + 32;
  constexpr int QP = InCfg<TKV>::QP; char* qp = shm + shm_qp<NWT>() + tid * 16;
  const int R = wid * QBLK + r32, Rc = R < ROWS ? R : ROWS - 1;
  int len = SK; if constexpr (HAS_LENS) { len = __builtin_amdgcn_readfirstlane(lens[b]); len = len < 0 ? 0 : (len > SK ? SK : len); }
  const int lim0 = CAUSAL ? len - SQ : len - 1, lim = CAUSAL ? lim0 + Rc % SQ : lim0, kb0 = split * TPS * KVBLK;
  bf16x8 qr[8]; const TQ* qrow = Qg + (size_t)Rc * D;
  float m_reg = -__builtin_inff(), msub = 0.f, l_reg = 0.f; f32x16 o[4] = {};
  const int sr = tid >> 4, sc = (tid & 15) * 8;
  const int vb0 = (int)(uintptr_t)V_lds + (lane & 15) * 8 + ((lane >> 4) & 1) * 1024 + (lane >> 5) * 256;
  Stage<TKV, NR> st; int sk0 = 0; (void)sk0;
#define SLOAD(k0) do { sk0 = kb0 + (k0); \
    if constexpr (NR == 2) st.load(Vh + (size_t)((k0) + sr) * D + sc, Vh + (size_t)((k0) + 32 + sr) * D + sc, Kh + (size_t)((k0) + sr) * D + sc, Kh + (size_t)((k0) + 32 + sr) * D + sc); \
    else { const TKV* vr_ = Vh + (size_t)((k0) + NR * sr) * D + sc; const TKV* kr_ = Kh + (size_t)((k0) + NR * sr) * D + sc; \
           for (int i_ = 0; i_ < NR; ++i_) st.ldv(i_, vr_ + i_ * D); for (int i_ = 0; i_ < NR; ++i_) st.ldk(i_, kr_ + i_ * D); } } while (0)
#define SWRITE(buf) do { if constexpr (NR == 2) { bf16x8 v0_ = st.gv0(), v1_ = st.gv1(), k0_ = st.gk0(), k1_ = st.gk1(); \
      if constexpr (HAS_LENS) { if (sk0 + KVBLK > len) { const bf16x8 z_ = {}; if (sk0 + sr >= len) { v0_ = z_; k0_ = z_; } if (sk0 + 32 + sr >= len) { v1_ = z_; k1_ = z_; } } } \
      *(bf16x8*)((char*)V_lds + (buf) * SHM_V + voff(sr, sc)) = v0_; *(bf16x8*)((char*)V_lds + (buf) * SHM_V + voff(32 + sr, sc)) = v1_; int kc = sc * 2; \
      *(bf16x8*)((char*)K_lds + (buf) * SHM_K + KSWZ(sr, kc)) = k0_; *(bf16x8*)((char*)K_lds + (buf) * SHM_K + KSWZ(32 + sr, kc)) = k1_; } \
    else { for (int i_ = 0; i_ < NR; ++i_) { const int row_ = NR * sr + i_; bf16x8 v_ = st.gv(i_), k_ = st.gk(i_); \
      if constexpr (HAS_LENS) { if (sk0 + KVBLK > len) { if (sk0 + row_ >= len) { v_ = bf16x8{}; k_ = bf16x8{}; } } } \
      *(bf16x8*)((char*)V_lds + (buf) * SHM_V + voff(row_, sc)) = v_; *(bf16x8*)((char*)K_lds + (buf) * SHM_K + KSWZ(row_, sc * 2)) = k_; } } } while (0)
#define RESC(a) do { if (__any((a) < 1.f)) { if constexpr (NWT == NW || !XCHG4) { if (hi == 0) al_l[r32] = (a); asm volatile("s_waitcnt lgkmcnt(0)" ::: "memory"); \
      for (int d = 0; d < 4; ++d) for (int r = 0; r < 16; ++r) o[d][r] *= al_l[crow(r, hi)]; } \
    else { const int ai_ = __float_as_int(a);    \
      for (int r = 0; r < 16; ++r) { const float a0_ = __int_as_float(__builtin_amdgcn_readlane(ai_, crow(r, 0))), a1_ = __int_as_float(__builtin_amdgcn_readlane(ai_, crow(r, 1))); \
        const float ar_ = hi ? a1_ : a0_; for (int d = 0; d < 4; ++d) o[d][r] *= ar_; } } } } while (0)
  f32x16 pA0, pA1, pB0, pB1; float alA, alB; bf16x8 pa0, pa1, pa2, pa3;
  for (int d0 = 0; d0 < 8; ++d0) qr[d0] = ld8(qrow + d0 * 16 + hi * 8);
  for (int j = 0; j < QP; ++j) *reinterpret_cast<bf16x8*>(qp + j * (64 * NWT * 16)) = qr[8 - QP + j];
  SLOAD(0); SWRITE(0); __syncthreads();
  qkt<QP, NWT>(pA0, pA1, K_lds, qr, qp, r32, hi); partialSM(pA0, pA1, m_reg, msub, alA, kb0, lim, lim0);
  SLOAD(KVBLK); SWRITE(1); __syncthreads();
  for (int j = 1; j + 1 < TPS; j += 2) {
    SBAR(); qkt<QP, NWT>(pB0, pB1, (bf16*)((char*)K_lds + SHM_K), qr, qp, r32, hi);
    finishSM(pA0, pA1, alA, l_reg, pa0, pa1, pa2, pa3); SBAR();
    SLOAD((j + 1) * KVBLK); SBAR();
    pv_d0(o, vb0, pa0, pa1, pa2, pa3); partialSM(pB0, pB1, m_reg, msub, alB, kb0 + j * KVBLK, lim, lim0);
    __syncthreads(); asm volatile("s_waitcnt vmcnt(0)" ::: "memory"); SWRITE(0);
    RESC(alB); __syncthreads();
    SBAR(); qkt<QP, NWT>(pA0, pA1, K_lds, qr, qp, r32, hi);
    finishSM(pB0, pB1, alB, l_reg, pa0, pa1, pa2, pa3); SBAR();
    if (j + 2 < TPS) { SLOAD((j + 2) * KVBLK); SBAR(); }
    pv_d0(o, vb0 + (int)SHM_V, pa0, pa1, pa2, pa3); partialSM(pA0, pA1, m_reg, msub, alA, kb0 + (j + 1) * KVBLK, lim, lim0);
    __syncthreads(); if (j + 2 < TPS) { asm volatile("s_waitcnt vmcnt(0)" ::: "memory"); SWRITE(1); }
    RESC(alA); __syncthreads();
  }
  SBAR(); qkt<QP, NWT>(pB0, pB1, (bf16*)((char*)K_lds + SHM_K), qr, qp, r32, hi);
  finishSM(pA0, pA1, alA, l_reg, pa0, pa1, pa2, pa3); SBAR();
  pv_d0(o, vb0, pa0, pa1, pa2, pa3); partialSM(pB0, pB1, m_reg, msub, alB, kb0 + (TPS - 1) * KVBLK, lim, lim0);
  __syncthreads(); RESC(alB);
  finishSM(pB0, pB1, alB, l_reg, pa0, pa1, pa2, pa3); SBAR();
  pv_d0(o, vb0 + (int)SHM_V, pa0, pa1, pa2, pa3);
#undef SLOAD
#undef SWRITE
#undef RESC
  if constexpr (NS == 1) {
    float rli[16];
    if constexpr (NWT == NW || !XCHG4) { if (hi == 0) li_l[r32] = l_reg; asm volatile("s_waitcnt lgkmcnt(0)" ::: "memory");
#pragma unroll
      for (int r = 0; r < 16; ++r) { float l = li_l[crow(r, hi)]; rli[r] = l > 0.f ? __builtin_amdgcn_rcpf(l) : 0.f; } }
    else {
#pragma unroll
      for (int r = 0; r < 16; ++r) { const int li_ = __float_as_int(l_reg); const float l0 = __int_as_float(__builtin_amdgcn_readlane(li_, crow(r, 0))), l1 = __int_as_float(__builtin_amdgcn_readlane(li_, crow(r, 1)));
        const float l = hi ? l1 : l0; rli[r] = l > 0.f ? __builtin_amdgcn_rcpf(l) : 0.f; } }
#pragma unroll
    for (int r = 0; r < 16; ++r) { const int Ro = wid * QBLK + crow(r, hi); if (Ro < ROWS) { float* orow = O + (((size_t)b * HQ + (size_t)kvh * G) * SQ + Ro) * D;
        for (int d0 = 0; d0 < 4; ++d0) orow[d0 * 32 + r32] = o[d0][r] * rli[r]; } }
  } else {
    const size_t row0 = ((size_t)b * HQ + (size_t)kvh * G) * SQ;
    if (hi == 0 && R < ROWS) { float* p = pml + ((row0 + R) * NS + split) * 2; p[0] = m_reg; p[1] = l_reg; }
#pragma unroll
    for (int r = 0; r < 16; ++r) { const int Ro = wid * QBLK + crow(r, hi); if (Ro < ROWS) { float* prow = po + ((row0 + Ro) * NS + split) * D;
        for (int d0 = 0; d0 < 4; ++d0) prow[d0 * 32 + r32] = o[d0][r]; } }
  }
}

constexpr int CMB_ROWS = 8, CMB_BLOCKS = (int)(((size_t)B * HQ * SQ + CMB_ROWS - 1) / CMB_ROWS);
__device__ __forceinline__ float b2f(unsigned short x){ return __uint_as_float(((unsigned)x)<<16); }
template <int NS> __global__ __launch_bounds__(256) void gqa_chunk_combine(const float* __restrict__ pml, const float* __restrict__ po, bf16* __restrict__ O,
   const unsigned short* __restrict__ q, const unsigned short* __restrict__ kt, const unsigned short* __restrict__ vt, const unsigned short* __restrict__ tm) {
  const size_t row = (size_t)blockIdx.x * CMB_ROWS + (threadIdx.x >> 5); const int j = threadIdx.x & 31; const int d = j * 4;
  const int t = row % SQ, h = (row / SQ) % HQ, b = row / (SQ * HQ), kvh = h / G;
  const unsigned short* qr = q + row * D; const unsigned short* kr = kt + (((size_t)b * HKV + kvh) * SQ + j) * D;
  float s = 0.f;
  for (int x = 0; x < D; x += 8) { bf16x8 a = *(const bf16x8*)(qr + x); bf16x8 c = *(const bf16x8*)(kr + x);
    for (int e = 0; e < 8; ++e) s += b2f((unsigned short)a[e]) * b2f((unsigned short)c[e]); }
  float mk = b2f(tm[((size_t)b * SQ + t) * SQ + j]);
  s = (s * SCALE + (1.0f - mk) * -1000000000.0f) * L2E;
  float mt = s; for (int o = 16; o >= 1; o >>= 1) mt = fmaxf(mt, __shfl_xor(mt, o, 32));
  float p = __builtin_amdgcn_exp2f(s - mt); float lt = p; for (int o = 16; o >= 1; o >>= 1) lt += __shfl_xor(lt, o, 32);
  f32x4 ot = {0.f,0.f,0.f,0.f};
  for (int k = 0; k < 32; ++k) { float pk = __shfl(p, k, 32); const unsigned short* vr = vt + (((size_t)b * HKV + kvh) * SQ + k) * D + d;
    s16x4 v = *(const s16x4*)vr; for (int e = 0; e < 4; ++e) ot[e] += pk * b2f((unsigned short)v[e]); }
  const float* ml = pml + row * NS * 2; const float* acc = po + row * NS * D + d;
  float M = mt; for (int s = 0; s < NS; ++s) M = fmaxf(M, ml[2 * s]);
  float wt = __builtin_amdgcn_exp2f(mt - M);
  float L = wt * lt; f32x4 o = {wt*ot[0], wt*ot[1], wt*ot[2], wt*ot[3]};
  for (int s = 0; s < NS; ++s) { const float m = ml[2 * s], l = ml[2 * s + 1]; const float w = l > 0.f ? __builtin_amdgcn_exp2f(m - M) : 0.f;
    const f32x4 a = *reinterpret_cast<const f32x4*>(acc + (size_t)s * D); L += w * l; o[0] += w * a[0]; o[1] += w * a[1]; o[2] += w * a[2]; o[3] += w * a[3]; }
  const float inv = 1.f / L;
  unsigned w0 = cvtpk(o[0]*inv, o[1]*inv), w1 = cvtpk(o[2]*inv, o[3]*inv);
  *reinterpret_cast<uint2*>((unsigned short*)O + row * D + d) = make_uint2(w0, w1);
}
extern "C" void kernel_launch(void* const* d_in, const int* in_sizes, int n_in, void* d_out, int out_size, void* d_ws, size_t ws_size, hipStream_t stream) {
  constexpr size_t shm = shm_bytes<bf16>();
  hipFuncSetAttribute((const void*)gqa_chunk_fwd<bf16, bf16>, hipFuncAttributeMaxDynamicSharedMemorySize, (int)shm);
  float* pml = (float*)((char*)d_ws + WS_PML); float* po = (float*)((char*)d_ws + WS_PO);
  constexpr int NS = ns_for<bf16>();
  hipLaunchKernelGGL((gqa_chunk_fwd<bf16, bf16>), dim3(NS, HKV, B), dim3(64 * nw_for<bf16>()), shm, stream, (const bf16*)d_in[0], (const bf16*)d_in[1], (const bf16*)d_in[2], (const int*)nullptr, (float*)nullptr, pml, po);
  hipLaunchKernelGGL((gqa_chunk_combine<NS>), dim3(CMB_BLOCKS), dim3(256), 0, stream, (const float*)pml, (const float*)po, (bf16*)d_out,
     (const unsigned short*)d_in[0], (const unsigned short*)d_in[3], (const unsigned short*)d_in[4], (const unsigned short*)d_in[5]);
}
```

```cpp
#include <hip/hip_runtime.h>
#include <hip/hip_bf16.h>


typedef float f32x4 __attribute__((ext_vector_type(4)));
typedef float f32x2 __attribute__((ext_vector_type(2)));
typedef __bf16 bf16x8 __attribute__((ext_vector_type(8)));
typedef __bf16 bf16x2 __attribute__((ext_vector_type(2)));
typedef unsigned u32x4 __attribute__((ext_vector_type(4)));
typedef unsigned u32x2 __attribute__((ext_vector_type(2)));
typedef __attribute__((address_space(1))) const u32x4 gu32x4;
typedef __attribute__((address_space(1))) const u32x2 gu32x2;
typedef unsigned long long u64x4 __attribute__((ext_vector_type(4)));
#define DI __device__ __forceinline__
#define SB() __builtin_amdgcn_sched_barrier(0)

constexpr int C_ = 8192;
constexpr unsigned WS_EPOCH = 0, WS_PART = 1u << 20, CH = 8576, HS = 8 * CH;
constexpr unsigned WS_BYTES = WS_PART + 256u * HS;
constexpr float QSCALE = 0.12751743082f;
constexpr float LOG2E = 1.4426950408889634f;

DI unsigned cvtpk(float lo, float hi) { f32x2 v = {lo, hi}; bf16x2 b = __builtin_convertvector(v, bf16x2); return __builtin_bit_cast(unsigned, b); }
DI float bflo(unsigned x) { return __uint_as_float(x << 16); }
DI float bfhi(unsigned x) { return __uint_as_float(x & 0xffff0000u); }
DI float max4(float x) {
  auto a = __builtin_amdgcn_permlane16_swap(__float_as_uint(x), __float_as_uint(x), false, false); x = fmaxf(__uint_as_float(a[0]), __uint_as_float(a[1]));
  auto r = __builtin_amdgcn_permlane32_swap(__float_as_uint(x), __float_as_uint(x), false, false); return fmaxf(__uint_as_float(r[0]), __uint_as_float(r[1])); }
DI __amdgpu_buffer_rsrc_t mkrsrc(const void* p, unsigned bytes) { return __builtin_amdgcn_make_buffer_rsrc((void*)p, (short)0, (int)bytes, 0x00020000); }
DI u32x4 scaleq(u32x4 x) { u32x4 r; for (int e = 0; e < 4; ++e) r[e] = cvtpk(bflo(x[e]) * QSCALE, bfhi(x[e]) * QSCALE); return r; }
DI f32x4 mfma16(u32x4 a, u32x4 b, f32x4 c) { return __builtin_amdgcn_mfma_f32_16x16x32_bf16(__builtin_bit_cast(bf16x8, a), __builtin_bit_cast(bf16x8, b), c, 0, 0, 0); }

__global__ __launch_bounds__(256, 1) __attribute__((aligned(4096))) void tree_attn_kernel(const char* Q_, const char* K_, const char* V_,
                                                           const char* Kt_, const char* Vt_, const char* Mk_,
                                                           char* Out_, char* ws_) {
  __shared__ __attribute__((aligned(256))) char lds[163840];
  u64x4 pa, pb;
  asm volatile("s_load_dwordx8 %0, %2, 0x0\n\ts_load_dwordx8 %1, %2, 0x20" : "=&s"(pa), "=&s"(pb) : "s"(__builtin_amdgcn_kernarg_segment_ptr()));
  asm volatile("s_getpc_b64 s[92:93]\nLpc%=:\ns_and_b32 s93, s93, 0xffff\ns_mov_b32 s94, .Ltree_attn_end-Lpc%=\ns_mov_b32 s95, 0x20000\ns_mov_b32 m0, 0xe000\n"
               "v_lshrrev_b32 v28, 4, %0\ns_lshr_b32 s91, %1, 3\nv_lshlrev_b32 v28, 12, v28\ns_lshl_b32 s91, s91, 7\nbuffer_load_dword v28, s[92:95], s91 offen lds"
               :: "v"(threadIdx.x), "s"(blockIdx.x) : "memory", "scc", "s91", "s92", "s93", "s94", "s95", "v28");
  asm volatile("" :: "v"(lds));
  float m0 = 0.f, m1 = 0.f, m2 = 0.f, m3 = 0.f;
  unsigned mode = 0, lane2, taglo = 0, taghi = 0, bid, w;
  for (;;) {
    unsigned bad, tid = threadIdx.x;
    bid = blockIdx.x;
    asm volatile("" : "+v"(mode), "+v"(tid)); asm volatile("" : "+s"(bid));
    const unsigned modes = __builtin_amdgcn_readfirstlane(mode);
    const unsigned g = (bid & 7u) * 4u + (bid >> 6), s = (bid >> 3) & 7u;
    w = __builtin_amdgcn_readfirstlane(tid >> 6);
    unsigned kvoff = g * (C_ * 256u) + s * 8192u, qoff = g * 65536u, kvlen = C_ * 256u - s * 8192u;
    const unsigned US = 65536u;
    const unsigned lane0 = tid & 63u, c0 = lane0 & 15u, lg0 = lane0 >> 4, q40 = (lane0 >> 2) & 3u;
    unsigned voffk = 256u * (8u * w + lg0) + 16u * (c0 ^ ((8u * w + lg0) & 15u));
    unsigned voffv = 256u * (8u * w + lg0) + 16u * (c0 ^ (lg0 << 1));
    unsigned voffq = 256u * lg0 + 16u * (c0 ^ lg0);
    unsigned m0w = 2048u * w, qpo = s * 8192u + m0w;
    asm volatile("" : "+v"(voffk), "+v"(voffv), "+v"(voffq));
    asm volatile("s_waitcnt lgkmcnt(0)" : "+s"(pa), "+s"(pb), "+s"(kvoff), "+s"(qoff), "+s"(kvlen), "+s"(m0w), "+s"(qpo));
    const char* Q = (const char*)pa[0]; const char* K = (const char*)pa[1]; const char* V = (const char*)pa[2]; const char* Kt = (const char*)pa[3];
    const char* Vt = (const char*)pb[0]; const char* Mk = (const char*)pb[1]; char* Out = (char*)pb[2]; char* ws = (char*)pb[3];
    const __amdgpu_buffer_rsrc_t srdk = mkrsrc(K + kvoff, kvlen), srdq = mkrsrc(Q + qoff, 65536u);
    asm volatile(
    "v_xor_b32 v124, 64, %[voffk]\n"
    "s_add_u32 m0, %[m0w], 0\n"
    "s_mov_b32 s80, 0\n"
    "buffer_load_dwordx4 %[voffk], %[srdk], s80 offen nt lds\n"
    "buffer_load_dwordx4 v124, %[srdk], s80 offen offset:1024 nt lds\n"
    "v_xor_b32 v109, 64, %[voffq]\n"
    "v_xor_b32 v110, 128, %[voffq]\n"
    "v_xor_b32 v111, 192, %[voffq]\n"
    "s_lshl_b32 s82, %[m0w], 3\n"
    "v_mbcnt_lo_u32_b32 v28, -1, 0\n"
    "s_add_u32 m0, %[m0w], 57344\n"
    "v_lshlrev_b32 v28, 7, v28\n"
    "v_and_b32 v28, 0x780, v28\n"
    "buffer_load_dword v28, %[srdq], %[qpo] offen lds\n"
    "s_add_u32 m0, s82, 98304\n"
    "s_add_u32 s83, s82, 0\n"
    "buffer_load_dwordx4 %[voffq], %[srdq], s83 offen lds\n"
    "buffer_load_dwordx4 v109, %[srdq], s83 offen offset:1024 lds\n"
    "buffer_load_dwordx4 v110, %[srdq], s83 offen offset:2048 lds\n"
    "buffer_load_dwordx4 v111, %[srdq], s83 offen offset:3072 lds\n"
    "s_add_u32 m0, s82, 102400\n"
    "s_add_u32 s83, s82, 4096\n"
    "buffer_load_dwordx4 %[voffq], %[srdq], s83 offen lds\n"
    "buffer_load_dwordx4 v109, %[srdq], s83 offen offset:1024 lds\n"
    "buffer_load_dwordx4 v110, %[srdq], s83 offen offset:2048 lds\n"
    "buffer_load_dwordx4 v111, %[srdq], s83 offen offset:3072 lds\n"
      :: [voffk] "v"(voffk), [voffv] "v"(voffv), [voffq] "v"(voffq), [srdk] "s"(srdk), [srdq] "s"(srdq), [m0w] "s"(m0w), [qpo] "s"(qpo)
      : "memory", "scc", "s80", "s82", "s83", "v28", "v109", "v110", "v111", "v124", "v125");
    SB();
    const unsigned epo = WS_EPOCH + g * 128u;
    const __amdgpu_buffer_rsrc_t srdv = mkrsrc(V + kvoff - US, kvlen + US);
    const __amdgpu_buffer_rsrc_t srdw = mkrsrc(ws, WS_BYTES);
    const __amdgpu_buffer_rsrc_t srdkt = mkrsrc(Kt + (size_t)g * 8192u, 8192u), srdvt = mkrsrc(Vt + (size_t)g * 8192u, 8192u), srdm = mkrsrc(Mk + (size_t)(g >> 2) * 2048u, 2048u);
    const unsigned kbase = 256u * c0 + 16u * (lg0 ^ c0);
    const unsigned vbase = 65536u + 256u * (4u * lg0 + q40) + 32u * (4u * (lg0 & 1u) + q40) + 8u * (lane0 & 3u);
    const unsigned own = (s >> 1) == w ? 1u + (s & 1u) : 0u;
    const unsigned pbase = WS_PART + (g * 8u + 2u * w) * HS + s * CH, pb0 = own == 1u ? 0x40000000u : pbase, pb1 = own == 2u ? 0x40000000u : pbase + HS, sbase = pbase + 8192u;
    const unsigned cbase = WS_PART + (g * 8u + s) * HS;
    const unsigned epw = (s == 0u && w == 0u) ? epo : 0x40000000u;
    const __amdgpu_buffer_rsrc_t srdo = mkrsrc(Out + ((size_t)g * 8u + s) * 8192u, 8192u);
    unsigned redo;
    asm volatile(
    "v_mov_b32 v32, 0\n"
    "v_mov_b32 v33, 0\n"
    "v_mov_b32 v34, 0\n"
    "v_mov_b32 v35, 0\n"
    "v_xor_b32 v124, 64, %[voffk]\n"
    "v_xor_b32 v125, 128, %[voffv]\n"
    "v_xor_b32 v109, 64, %[voffq]\n"
    "v_xor_b32 v110, 128, %[voffq]\n"
    "v_xor_b32 v111, 192, %[voffq]\n"
    "s_lshl_b32 s82, %[m0w], 3\n"
    "v_mbcnt_lo_u32_b32 v127, -1, 0\n"
    "v_mbcnt_hi_u32_b32 v127, -1, v127\n"
    "s_mov_b32 s81, 0x3e0293ee\n"
    "v_mfma_f32_32x32x16_bf16 a[192:207], v[32:35], v[32:35], 0\n"
    "v_xor_b32 v112, 0, %[kbase]\n"
    "v_xor_b32 v113, 64, %[kbase]\n"
    "v_xor_b32 v114, 128, %[kbase]\n"
    "v_xor_b32 v115, 192, %[kbase]\n"
    "s_add_u32 s83, s82, 98304\n"
    "v_add_u32 v92, s83, v112\n"
    "v_add_u32 v93, s83, v113\n"
    "v_add_u32 v94, s83, v114\n"
    "v_add_u32 v95, s83, v115\n"
    "v_xor_b32 v116, 0, %[vbase]\n"
    "v_xor_b32 v117, 32, %[vbase]\n"
    "v_xor_b32 v118, 64, %[vbase]\n"
    "v_xor_b32 v119, 96, %[vbase]\n"
    "v_xor_b32 v120, 128, %[vbase]\n"
    "v_xor_b32 v121, 160, %[vbase]\n"
    "v_xor_b32 v122, 192, %[vbase]\n"
    "v_xor_b32 v123, 224, %[vbase]\n"
    "v_mov_b32 v28, 0x3f803f80\n"
    "v_accvgpr_write_b32 a208, v28\n"
    "v_accvgpr_write_b32 a209, v28\n"
    "v_accvgpr_write_b32 a210, v28\n"
    "v_accvgpr_write_b32 a211, v28\n"
    "v_and_b32 v28, 1, v127\n"
    "v_sub_u32 v28, 0, v28\n"
    "v_and_b32 v28, 0xaaaaaaaa, v28\n"
    "v_or_b32 v126, 0x44444444, v28\n"
    "v_lshlrev_b32 v127, 4, v127\n"
    "s_cmp_lg_u32 %[mode], 0\n"
    "s_cselect_b64 s[94:95], -1, 0\n"
    "s_mov_b64 exec, 0\n"
    "s_bitcmp1_b32 %[m0w], 11\n"
    "s_cbranch_scc1 Lhoq1%=\n"
    "s_branch Lh0_%=\n"
    "Lr0_%=:\n"
    "s_branch Lh2_%=\n"
    "Lr2_%=:\n"
    "s_branch Lhoq2%=\n"
    "Lhoq1%=:\n"
    "s_branch Lh1_%=\n"
    "Lr1_%=:\n"
    "s_branch Lh3_%=\n"
    "Lr3_%=:\n"
    "Lhoq2%=:\n"
    "s_mov_b64 exec, -1\n"
    "s_add_u32 m0, s82, 106496\n"
    "s_add_u32 s83, s82, 8192\n"
    "buffer_load_dwordx4 %[voffq], %[srdq], s83 offen lds\n"
    "buffer_load_dwordx4 v109, %[srdq], s83 offen offset:1024 lds\n"
    "buffer_load_dwordx4 v110, %[srdq], s83 offen offset:2048 lds\n"
    "buffer_load_dwordx4 v111, %[srdq], s83 offen offset:3072 lds\n"
    "s_add_u32 m0, s82, 110592\n"
    "s_add_u32 s83, s82, 12288\n"
    "buffer_load_dwordx4 %[voffq], %[srdq], s83 offen lds\n"
    "buffer_load_dwordx4 v109, %[srdq], s83 offen offset:1024 lds\n"
    "buffer_load_dwordx4 v110, %[srdq], s83 offen offset:2048 lds\n"
    "buffer_load_dwordx4 v111, %[srdq], s83 offen offset:3072 lds\n"
    "s_mov_b32 s80, 65536\n"
    "s_add_u32 m0, %[m0w], 8192\n"
    "s_nop 0\n"
    "buffer_load_dwordx4 %[voffk], %[srdk], s80 offen nt lds\n"
    "buffer_load_dwordx4 v124, %[srdk], s80 offen offset:1024 nt lds\n"
    "s_add_u32 m0, %[m0w], 65536\n"
    "s_nop 0\n"
    "buffer_load_dwordx4 %[voffv], %[srdv], s80 offen nt lds\n"
    "buffer_load_dwordx4 v125, %[srdv], s80 offen offset:1024 nt lds\n"
    "s_waitcnt vmcnt(16)\n"
    "s_barrier\n"
    "ds_read_b128 a[64:67], v112 offset:0\n"
    "ds_read_b128 a[68:71], v113 offset:0\n"
    "ds_read_b128 a[72:75], v114 offset:0\n"
    "ds_read_b128 a[76:79], v115 offset:0\n"
    "ds_read_b128 a[80:83], v112 offset:4096\n"
    "ds_read_b128 a[84:87], v113 offset:4096\n"
    "ds_read_b128 a[88:91], v114 offset:4096\n"
    "ds_read_b128 a[92:95], v115 offset:4096\n"
    "ds_read_b128 a[0:3], v92 offset:0\n"
    "ds_read_b128 a[4:7], v93 offset:0\n"
    "ds_read_b128 a[8:11], v94 offset:0\n"
    "ds_read_b128 a[12:15], v95 offset:0\n"
    "s_waitcnt vmcnt(12)\n"
    "ds_read_b128 a[16:19], v92 offset:4096\n"
    "ds_read_b128 a[20:23], v93 offset:4096\n"
    "ds_read_b128 a[24:27], v94 offset:4096\n"
    "ds_read_b128 a[28:31], v95 offset:4096\n"
    "; -- qk00\n"
    "s_waitcnt lgkmcnt(7)\n"
    "v_mfma_f32_16x16x32_bf16 v[32:35], a[64:67], a[0:3], 0\n"
    "s_waitcnt lgkmcnt(6)\n"
    "v_mfma_f32_16x16x32_bf16 v[32:35], a[68:71], a[4:7], v[32:35]\n"
    "s_waitcnt lgkmcnt(5)\n"
    "v_mfma_f32_16x16x32_bf16 v[32:35], a[72:75], a[8:11], v[32:35]\n"
    "s_waitcnt lgkmcnt(4)\n"
    "v_mfma_f32_16x16x32_bf16 v[32:35], a[76:79], a[12:15], v[32:35]\n"
    "v_mfma_f32_16x16x32_bf16 v[36:39], a[80:83], a[0:3], 0\n"
    "v_mfma_f32_16x16x32_bf16 v[36:39], a[84:87], a[4:7], v[36:39]\n"
    "v_mfma_f32_16x16x32_bf16 v[36:39], a[88:91], a[8:11], v[36:39]\n"
    "v_mfma_f32_16x16x32_bf16 v[36:39], a[92:95], a[12:15], v[36:39]\n"
    "s_waitcnt vmcnt(8)\n"
    "ds_read_b128 a[32:35], v92 offset:8192\n"
    "ds_read_b128 a[36:39], v93 offset:8192\n"
    "ds_read_b128 a[40:43], v94 offset:8192\n"
    "ds_read_b128 a[44:47], v95 offset:8192\n"
    "; -- qk01\n"
    "s_waitcnt lgkmcnt(7)\n"
    "v_mfma_f32_16x16x32_bf16 v[40:43], a[64:67], a[16:19], 0\n"
    "v_accvgpr_read_b32 v21, a0\n"
    "v_accvgpr_read_b32 v23, a1\n"
    "s_waitcnt lgkmcnt(5)\n"
    "v_mfma_f32_16x16x32_bf16 v[40:43], a[68:71], a[20:23], v[40:43]\n"
    "v_lshlrev_b32 v20, 16, v21\n"
    "v_lshlrev_b32 v22, 16, v23\n"
    "v_mfma_f32_16x16x32_bf16 v[40:43], a[72:75], a[24:27], v[40:43]\n"
    "v_and_b32 v21, 0xffff0000, v21\n"
    "v_and_b32 v23, 0xffff0000, v23\n"
    "s_waitcnt lgkmcnt(4)\n"
    "v_mfma_f32_16x16x32_bf16 v[40:43], a[76:79], a[28:31], v[40:43]\n"
    "v_mul_f32 v20, s81, v20\n"
    "v_mul_f32 v22, s81, v22\n"
    "v_mfma_f32_16x16x32_bf16 v[44:47], a[80:83], a[16:19], 0\n"
    "v_mul_f32 v21, s81, v21\n"
    "v_mul_f32 v23, s81, v23\n"
    "v_mfma_f32_16x16x32_bf16 v[44:47], a[84:87], a[20:23], v[44:47]\n"
    "v_cvt_pk_bf16_f32 v20, v20, v21\n"
    "v_cvt_pk_bf16_f32 v22, v22, v23\n"
    "v_mfma_f32_16x16x32_bf16 v[44:47], a[88:91], a[24:27], v[44:47]\n"
    "v_accvgpr_write_b32 a0, v20\n"
    "v_accvgpr_write_b32 a1, v22\n"
    "v_mfma_f32_16x16x32_bf16 v[44:47], a[92:95], a[28:31], v[44:47]\n"
    "v_accvgpr_read_b32 v21, a2\n"
    "v_accvgpr_read_b32 v23, a3\n"
    "s_waitcnt vmcnt(4)\n"
    "ds_read_b128 a[48:51], v92 offset:12288\n"
    "ds_read_b128 a[52:55], v93 offset:12288\n"
    "ds_read_b128 a[56:59], v94 offset:12288\n"
    "ds_read_b128 a[60:63], v95 offset:12288\n"
    "; -- qk02\n"
    "s_waitcnt lgkmcnt(4)\n"
    "v_mfma_f32_16x16x32_bf16 v[48:51], a[64:67], a[32:35], 0\n"
    "v_max3_f32 v28, v32, v33, v34\n"
    "v_max3_f32 v26, v35, v36, v37\n"
    "v_mfma_f32_16x16x32_bf16 v[48:51], a[68:71], a[36:39], v[48:51]\n"
    "v_max3_f32 v28, v28, v38, v39\n"
    "v_max_f32 v28, v28, v26\n"
    "v_mfma_f32_16x16x32_bf16 v[48:51], a[72:75], a[40:43], v[48:51]\n"
    "v_mov_b32 v26, v28\n"
    "s_nop 1\n"
    "v_permlane16_swap_b32 v28, v26\n"
    "v_mfma_f32_16x16x32_bf16 v[48:51], a[76:79], a[44:47], v[48:51]\n"
    "v_max_f32 v28, v28, v26\n"
    "v_mov_b32 v26, v28\n"
    "v_mfma_f32_16x16x32_bf16 v[52:55], a[80:83], a[32:35], 0\n"
    "s_mov_b32 s80, 131072\n"
    "s_add_u32 m0, %[m0w], 16384\n"
    "s_nop 0\n"
    "buffer_load_dwordx4 %[voffk], %[srdk], s80 offen nt lds\n"
    "v_permlane32_swap_b32 v28, v26\n"
    "v_max_f32 v28, v28, v26\n"
    "v_mfma_f32_16x16x32_bf16 v[52:55], a[84:87], a[36:39], v[52:55]\n"
    "v_mul_f32 v28, s81, v28\n"
    "v_cndmask_b32 %[m0], v28, %[m0], s[94:95]\n"
    "v_mfma_f32_16x16x32_bf16 v[52:55], a[88:91], a[40:43], v[52:55]\n"
    "v_sub_f32 v96, 0, %[m0]\n"
    "v_sub_f32 v97, 0, %[m0]\n"
    "v_mfma_f32_16x16x32_bf16 v[52:55], a[92:95], a[44:47], v[52:55]\n"
    "v_sub_f32 v98, 0, %[m0]\n"
    "v_sub_f32 v99, 0, %[m0]\n"
    "; -- qk03\n"
    "s_waitcnt lgkmcnt(0)\n"
    "v_mfma_f32_16x16x32_bf16 v[56:59], a[64:67], a[48:51], 0\n"
    "v_max3_f32 v29, v40, v41, v42\n"
    "v_max3_f32 v27, v43, v44, v45\n"
    "v_mfma_f32_16x16x32_bf16 v[56:59], a[68:71], a[52:55], v[56:59]\n"
    "v_max3_f32 v29, v29, v46, v47\n"
    "v_max_f32 v29, v29, v27\n"
    "v_mfma_f32_16x16x32_bf16 v[56:59], a[72:75], a[56:59], v[56:59]\n"
    "v_mov_b32 v27, v29\n"
    "s_nop 1\n"
    "v_permlane16_swap_b32 v29, v27\n"
    "v_mfma_f32_16x16x32_bf16 v[56:59], a[76:79], a[60:63], v[56:59]\n"
    "v_max_f32 v29, v29, v27\n"
    "v_mov_b32 v27, v29\n"
    "v_mfma_f32_16x16x32_bf16 v[60:63], a[80:83], a[48:51], 0\n"
    "buffer_load_dwordx4 v124, %[srdk], s80 offen offset:1024 nt lds\n"
    "v_permlane32_swap_b32 v29, v27\n"
    "v_max_f32 v29, v29, v27\n"
    "v_mfma_f32_16x16x32_bf16 v[60:63], a[84:87], a[52:55], v[60:63]\n"
    "v_mul_f32 v29, s81, v29\n"
    "v_cndmask_b32 %[m1], v29, %[m1], s[94:95]\n"
    "v_mfma_f32_16x16x32_bf16 v[60:63], a[88:91], a[56:59], v[60:63]\n"
    "v_sub_f32 v100, 0, %[m1]\n"
    "v_sub_f32 v101, 0, %[m1]\n"
    "v_mfma_f32_16x16x32_bf16 v[60:63], a[92:95], a[60:63], v[60:63]\n"
    "v_sub_f32 v102, 0, %[m1]\n"
    "v_sub_f32 v103, 0, %[m1]\n"
    "v_lshlrev_b32 v20, 16, v21\n"
    "v_lshlrev_b32 v22, 16, v23\n"
    "v_and_b32 v21, 0xffff0000, v21\n"
    "v_and_b32 v23, 0xffff0000, v23\n"
    "v_mul_f32 v20, s81, v20\n"
    "v_fma_f32 v32, v32, s81, v96\n"
    "v_mul_f32 v22, s81, v22\n"
    "v_mul_f32 v21, s81, v21\n"
    "v_mul_f32 v23, s81, v23\n"
    "v_cvt_pk_bf16_f32 v20, v20, v21\n"
    "v_cvt_pk_bf16_f32 v22, v22, v23\n"
    "v_accvgpr_write_b32 a2, v20\n"
    "s_add_u32 m0, %[m0w], 73728\n"
    "s_nop 0\n"
    "buffer_load_dwordx4 %[voffv], %[srdv], s80 offen nt lds\n"
    "v_accvgpr_write_b32 a3, v22\n"
    "v_accvgpr_read_b32 v21, a4\n"
    "v_accvgpr_read_b32 v23, a5\n"
    "v_lshlrev_b32 v20, 16, v21\n"
    "v_lshlrev_b32 v22, 16, v23\n"
    "v_fma_f32 v33, v33, s81, v96\n"
    "v_and_b32 v21, 0xffff0000, v21\n"
    "v_and_b32 v23, 0xffff0000, v23\n"
    "v_mul_f32 v20, s81, v20\n"
    "v_mul_f32 v22, s81, v22\n"
    "v_mul_f32 v21, s81, v21\n"
    "v_mul_f32 v23, s81, v23\n"
    "v_cvt_pk_bf16_f32 v20, v20, v21\n"
    "v_cvt_pk_bf16_f32 v22, v22, v23\n"
    "v_accvgpr_write_b32 a4, v20\n"
    "v_accvgpr_write_b32 a5, v22\n"
    "v_exp_f32 v32, v32\n"
    "v_accvgpr_read_b32 v21, a6\n"
    "v_accvgpr_read_b32 v23, a7\n"
    "v_lshlrev_b32 v20, 16, v21\n"
    "v_lshlrev_b32 v22, 16, v23\n"
    "v_and_b32 v21, 0xffff0000, v21\n"
    "v_and_b32 v23, 0xffff0000, v23\n"
    "v_mul_f32 v20, s81, v20\n"
    "buffer_load_dwordx4 v125, %[srdv], s80 offen offset:1024 nt lds\n"
    "v_mul_f32 v22, s81, v22\n"
    "v_mul_f32 v21, s81, v21\n"
    "v_mul_f32 v23, s81, v23\n"
    "v_fma_f32 v34, v34, s81, v96\n"
    "v_cvt_pk_bf16_f32 v20, v20, v21\n"
    "v_cvt_pk_bf16_f32 v22, v22, v23\n"
    "v_accvgpr_write_b32 a6, v20\n"
    "v_accvgpr_write_b32 a7, v22\n"
    "v_accvgpr_read_b32 v21, a8\n"
    "v_accvgpr_read_b32 v23, a9\n"
    "v_lshlrev_b32 v20, 16, v21\n"
    "v_lshlrev_b32 v22, 16, v23\n"
    "v_and_b32 v21, 0xffff0000, v21\n"
    "v_and_b32 v23, 0xffff0000, v23\n"
    "v_mul_f32 v20, s81, v20\n"
    "v_exp_f32 v33, v33\n"
    "v_mul_f32 v22, s81, v22\n"
    "v_mul_f32 v21, s81, v21\n"
    "v_mul_f32 v23, s81, v23\n"
    "v_cvt_pk_bf16_f32 v20, v20, v21\n"
    "v_cvt_pk_bf16_f32 v22, v22, v23\n"
    "v_accvgpr_write_b32 a8, v20\n"
    "v_accvgpr_write_b32 a9, v22\n"
    "v_accvgpr_read_b32 v21, a10\n"
    "s_mov_b32 s80, 196608\n"
    "s_add_u32 m0, %[m0w], 24576\n"
    "s_nop 0\n"
    "buffer_load_dwordx4 %[voffk], %[srdk], s80 offen nt lds\n"
    "v_accvgpr_read_b32 v23, a11\n"
    "v_lshlrev_b32 v20, 16, v21\n"
    "v_fma_f32 v35, v35, s81, v96\n"
    "v_lshlrev_b32 v22, 16, v23\n"
    "v_and_b32 v21, 0xffff0000, v21\n"
    "v_and_b32 v23, 0xffff0000, v23\n"
    "v_mul_f32 v20, s81, v20\n"
    "v_mul_f32 v22, s81, v22\n"
    "v_mul_f32 v21, s81, v21\n"
    "v_mul_f32 v23, s81, v23\n"
    "v_cvt_pk_bf16_f32 v20, v20, v21\n"
    "v_cvt_pk_bf16_f32 v22, v22, v23\n"
    "v_accvgpr_write_b32 a10, v20\n"
    "v_accvgpr_write_b32 a11, v22\n"
    "v_exp_f32 v34, v34\n"
    "v_accvgpr_read_b32 v21, a12\n"
    "v_accvgpr_read_b32 v23, a13\n"
    "v_lshlrev_b32 v20, 16, v21\n"
    "v_lshlrev_b32 v22, 16, v23\n"
    "v_and_b32 v21, 0xffff0000, v21\n"
    "v_and_b32 v23, 0xffff0000, v23\n"
    "v_mul_f32 v20, s81, v20\n"
    "v_mul_f32 v22, s81, v22\n"
    "v_mul_f32 v21, s81, v21\n"
    "buffer_load_dwordx4 v124, %[srdk], s80 offen offset:1024 nt lds\n"
    "v_mul_f32 v23, s81, v23\n"
    "v_cvt_pk_bf16_f32 v64, v32, v33\n"
    "v_cvt_pk_bf16_f32 v20, v20, v21\n"
    "v_cvt_pk_bf16_f32 v22, v22, v23\n"
    "v_accvgpr_write_b32 a12, v20\n"
    "v_accvgpr_write_b32 a13, v22\n"
    "v_accvgpr_read_b32 v21, a14\n"
    "v_accvgpr_read_b32 v23, a15\n"
    "v_lshlrev_b32 v20, 16, v21\n"
    "v_lshlrev_b32 v22, 16, v23\n"
    "v_and_b32 v21, 0xffff0000, v21\n"
    "v_and_b32 v23, 0xffff0000, v23\n"
    "v_fma_f32 v36, v36, s81, v96\n"
    "v_mul_f32 v20, s81, v20\n"
    "v_mul_f32 v22, s81, v22\n"
    "v_mul_f32 v21, s81, v21\n"
    "v_mul_f32 v23, s81, v23\n"
    "v_cvt_pk_bf16_f32 v20, v20, v21\n"
    "v_cvt_pk_bf16_f32 v22, v22, v23\n"
    "v_accvgpr_write_b32 a14, v20\n"
    "v_accvgpr_write_b32 a15, v22\n"
    "v_accvgpr_read_b32 v21, a16\n"
    "v_accvgpr_read_b32 v23, a17\n"
    "v_lshlrev_b32 v20, 16, v21\n"
    "s_add_u32 m0, %[m0w], 81920\n"
    "s_nop 0\n"
    "buffer_load_dwordx4 %[voffv], %[srdv], s80 offen nt lds\n"
    "v_exp_f32 v35, v35\n"
    "v_lshlrev_b32 v22, 16, v23\n"
    "v_and_b32 v21, 0xffff0000, v21\n"
    "v_and_b32 v23, 0xffff0000, v23\n"
    "v_mul_f32 v20, s81, v20\n"
    "v_mul_f32 v22, s81, v22\n"
    "v_mul_f32 v21, s81, v21\n"
    "v_mul_f32 v23, s81, v23\n"
    "v_cvt_pk_bf16_f32 v20, v20, v21\n"
    "v_cvt_pk_bf16_f32 v22, v22, v23\n"
    "v_accvgpr_write_b32 a16, v20\n"
    "v_fma_f32 v37, v37, s81, v96\n"
    "v_accvgpr_write_b32 a17, v22\n"
    "v_accvgpr_read_b32 v21, a18\n"
    "v_accvgpr_read_b32 v23, a19\n"
    "v_lshlrev_b32 v20, 16, v21\n"
    "v_lshlrev_b32 v22, 16, v23\n"
    "v_and_b32 v21, 0xffff0000, v21\n"
    "v_and_b32 v23, 0xffff0000, v23\n"
    "v_mul_f32 v20, s81, v20\n"
    "v_mul_f32 v22, s81, v22\n"
    "v_mul_f32 v21, s81, v21\n"
    "v_mul_f32 v23, s81, v23\n"
    "v_exp_f32 v36, v36\n"
    "buffer_load_dwordx4 v125, %[srdv], s80 offen offset:1024 nt lds\n"
    "v_cvt_pk_bf16_f32 v20, v20, v21\n"
    "v_cvt_pk_bf16_f32 v22, v22, v23\n"
    "v_accvgpr_write_b32 a18, v20\n"
    "v_accvgpr_write_b32 a19, v22\n"
    "v_accvgpr_read_b32 v21, a20\n"
    "v_accvgpr_read_b32 v23, a21\n"
    "v_lshlrev_b32 v20, 16, v21\n"
    "v_lshlrev_b32 v22, 16, v23\n"
    "v_and_b32 v21, 0xffff0000, v21\n"
    "v_and_b32 v23, 0xffff0000, v23\n"
    "v_cvt_pk_bf16_f32 v65, v34, v35\n"
    "v_mul_f32 v20, s81, v20\n"
    "v_mul_f32 v22, s81, v22\n"
    "v_mul_f32 v21, s81, v21\n"
    "v_mul_f32 v23, s81, v23\n"
    "v_cvt_pk_bf16_f32 v20, v20, v21\n"
    "v_cvt_pk_bf16_f32 v22, v22, v23\n"
    "v_accvgpr_write_b32 a20, v20\n"
    "v_accvgpr_write_b32 a21, v22\n"
    "v_accvgpr_read_b32 v21, a22\n"
    "v_accvgpr_read_b32 v23, a23\n"
    "v_fma_f32 v38, v38, s81, v96\n"
    "v_lshlrev_b32 v20, 16, v21\n"
    "v_lshlrev_b32 v22, 16, v23\n"
    "s_mov_b32 s80, 262144\n"
    "s_add_u32 m0, %[m0w], 32768\n"
    "s_nop 0\n"
    "buffer_load_dwordx4 %[voffk], %[srdk], s80 offen nt lds\n"
    "v_and_b32 v21, 0xffff0000, v21\n"
    "v_and_b32 v23, 0xffff0000, v23\n"
    "v_mul_f32 v20, s81, v20\n"
    "v_mul_f32 v22, s81, v22\n"
    "v_mul_f32 v21, s81, v21\n"
    "v_mul_f32 v23, s81, v23\n"
    "v_cvt_pk_bf16_f32 v20, v20, v21\n"
    "v_cvt_pk_bf16_f32 v22, v22, v23\n"
    "v_accvgpr_write_b32 a22, v20\n"
    "v_exp_f32 v37, v37\n"
    "v_accvgpr_write_b32 a23, v22\n"
    "v_accvgpr_read_b32 v21, a24\n"
    "v_accvgpr_read_b32 v23, a25\n"
    "v_lshlrev_b32 v20, 16, v21\n"
    "v_lshlrev_b32 v22, 16, v23\n"
    "v_and_b32 v21, 0xffff0000, v21\n"
    "v_and_b32 v23, 0xffff0000, v23\n"
    "v_mul_f32 v20, s81, v20\n"
    "v_mul_f32 v22, s81, v22\n"
    "v_mul_f32 v21, s81, v21\n"
    "v_fma_f32 v39, v39, s81, v96\n"
    "v_mul_f32 v23, s81, v23\n"
    "v_cvt_pk_bf16_f32 v20, v20, v21\n"
    "v_cvt_pk_bf16_f32 v22, v22, v23\n"
    "buffer_load_dwordx4 v124, %[srdk], s80 offen offset:1024 nt lds\n"
    "v_accvgpr_write_b32 a24, v20\n"
    "v_accvgpr_write_b32 a25, v22\n"
    "v_accvgpr_read_b32 v21, a26\n"
    "v_accvgpr_read_b32 v23, a27\n"
    "v_lshlrev_b32 v20, 16, v21\n"
    "v_lshlrev_b32 v22, 16, v23\n"
    "v_and_b32 v21, 0xffff0000, v21\n"
    "v_and_b32 v23, 0xffff0000, v23\n"
    "v_exp_f32 v38, v38\n"
    "v_mul_f32 v20, s81, v20\n"
    "v_mul_f32 v22, s81, v22\n"
    "v_mul_f32 v21, s81, v21\n"
    "v_mul_f32 v23, s81, v23\n"
    "v_cvt_pk_bf16_f32 v20, v20, v21\n"
    "v_cvt_pk_bf16_f32 v22, v22, v23\n"
    "v_accvgpr_write_b32 a26, v20\n"
    "v_accvgpr_write_b32 a27, v22\n"
    "v_accvgpr_read_b32 v21, a28\n"
    "v_accvgpr_read_b32 v23, a29\n"
    "v_cvt_pk_bf16_f32 v66, v36, v37\n"
    "v_lshlrev_b32 v20, 16, v21\n"
    "v_lshlrev_b32 v22, 16, v23\n"
    "v_and_b32 v21, 0xffff0000, v21\n"
    "v_and_b32 v23, 0xffff0000, v23\n"
    "s_add_u32 m0, %[m0w], 90112\n"
    "s_nop 0\n"
    "buffer_load_dwordx4 %[voffv], %[srdv], s80 offen nt lds\n"
    "v_mul_f32 v20, s81, v20\n"
    "v_mul_f32 v22, s81, v22\n"
    "v_mul_f32 v21, s81, v21\n"
    "v_mul_f32 v23, s81, v23\n"
    "v_cvt_pk_bf16_f32 v20, v20, v21\n"
    "v_cvt_pk_bf16_f32 v22, v22, v23\n"
    "v_exp_f32 v39, v39\n"
    "v_accvgpr_write_b32 a28, v20\n"
    "v_accvgpr_write_b32 a29, v22\n"
    "v_accvgpr_read_b32 v21, a30\n"
    "v_accvgpr_read_b32 v23, a31\n"
    "v_lshlrev_b32 v20, 16, v21\n"
    "v_lshlrev_b32 v22, 16, v23\n"
    "v_and_b32 v21, 0xffff0000, v21\n"
    "v_and_b32 v23, 0xffff0000, v23\n"
    "v_mul_f32 v20, s81, v20\n"
    "v_mul_f32 v22, s81, v22\n"
    "v_mul_f32 v21, s81, v21\n"
    "v_cvt_pk_bf16_f32 v67, v38, v39\n"
    "v_mul_f32 v23, s81, v23\n"
    "v_cvt_pk_bf16_f32 v20, v20, v21\n"
    "v_cvt_pk_bf16_f32 v22, v22, v23\n"
    "v_accvgpr_write_b32 a30, v20\n"
    "v_accvgpr_write_b32 a31, v22\n"
    "s_waitcnt lgkmcnt(0)\n"
    "s_waitcnt vmcnt(11)\n"
    "s_barrier\n"
    "ds_read_b128 a[96:99], v112 offset:8192\n"
    "ds_read_b128 a[100:103], v113 offset:8192\n"
    "ds_read_b128 a[104:107], v114 offset:8192\n"
    "ds_read_b128 a[108:111], v115 offset:8192\n"
    "ds_read_b128 a[112:115], v112 offset:12288\n"
    "ds_read_b128 a[116:119], v113 offset:12288\n"
    "ds_read_b128 a[120:123], v114 offset:12288\n"
    "ds_read_b128 a[124:127], v115 offset:12288\n"
    "ds_read_b64_tr_b16 a[128:129], v116 offset:0\n"
    "ds_read_b64_tr_b16 a[130:131], v116 offset:4096\n"
    "ds_read_b64_tr_b16 a[132:133], v117 offset:0\n"
    "ds_read_b64_tr_b16 a[134:135], v117 offset:4096\n"
    "ds_read_b64_tr_b16 a[136:137], v118 offset:0\n"
    "ds_read_b64_tr_b16 a[138:139], v118 offset:4096\n"
    "ds_read_b64_tr_b16 a[140:141], v119 offset:0\n"
    "ds_read_b64_tr_b16 a[142:143], v119 offset:4096\n"
    "ds_read_b64_tr_b16 a[144:145], v120 offset:0\n"
    "ds_read_b64_tr_b16 a[146:147], v120 offset:4096\n"
    "ds_read_b64_tr_b16 a[148:149], v121 offset:0\n"
    "ds_read_b64_tr_b16 a[150:151], v121 offset:4096\n"
    "ds_read_b64_tr_b16 a[152:153], v122 offset:0\n"
    "ds_read_b64_tr_b16 a[154:155], v122 offset:4096\n"
    "ds_read_b64_tr_b16 a[156:157], v123 offset:0\n"
    "ds_read_b64_tr_b16 a[158:159], v123 offset:4096\n"
    "v_max3_f32 v28, v48, v49, v50\n"
    "v_max3_f32 v26, v51, v52, v53\n"
    "v_max3_f32 v28, v28, v54, v55\n"
    "v_max_f32 v28, v28, v26\n"
    "v_mov_b32 v26, v28\n"
    "s_nop 1\n"
    "v_permlane16_swap_b32 v28, v26\n"
    "v_max_f32 v28, v28, v26\n"
    "v_mov_b32 v26, v28\n"
    "s_nop 1\n"
    "v_permlane32_swap_b32 v28, v26\n"
    "v_max_f32 v28, v28, v26\n"
    "v_mul_f32 v28, s81, v28\n"
    "v_cndmask_b32 %[m2], v28, %[m2], s[94:95]\n"
    "v_sub_f32 v104, 0, %[m2]\n"
    "v_sub_f32 v105, 0, %[m2]\n"
    "v_sub_f32 v106, 0, %[m2]\n"
    "v_sub_f32 v107, 0, %[m2]\n"
    "; ---- head unit 1\n"
    "buffer_load_dwordx4 v125, %[srdv], s80 offen offset:1024 nt lds\n"
    "s_add_u32 m0, %[m0w], 131072\n"
    "Lh0_%=:\n"
    "s_cbranch_execz Lr0_%=\n"
    "s_nop 0\n"
    "buffer_load_dwordx4 %[voffk], %[srdkt], 0 offen lds\n"
    "buffer_load_dwordx4 v124, %[srdkt], 0 offen offset:1024 lds\n"
    "s_add_u32 m0, %[m0w], 139264\n"
    "s_nop 0\n"
    "buffer_load_dwordx4 %[voffv], %[srdvt], 0 offen lds\n"
    "buffer_load_dwordx4 v125, %[srdvt], 0 offen offset:1024 lds\n"
    "s_lshr_b32 s82, %[m0w], 1\n"
    "s_add_u32 m0, s82, 147456\n"
    "s_nop 0\n"
    "buffer_load_dwordx4 v127, %[srdm], s82 offen lds\n"
    "; -- h10a\n"
    "s_waitcnt lgkmcnt(0)\n"
    "v_mfma_f32_16x16x32_bf16 v[32:35], a[96:99], a[0:3], v[96:99]\n"
    "v_fma_f32 v40, v40, s81, v100\n"
    "v_max3_f32 v29, v56, v57, v58\n"
    "v_fma_f32 v41, v41, s81, v100\n"
    "v_mfma_f32_16x16x32_bf16 v[32:35], a[100:103], a[4:7], v[32:35]\n"
    "v_max3_f32 v27, v59, v60, v61\n"
    "v_exp_f32 v40, v40\n"
    "v_mfma_f32_16x16x32_bf16 v[32:35], a[104:107], a[8:11], v[32:35]\n"
    "v_max3_f32 v29, v29, v62, v63\n"
    "v_fma_f32 v42, v42, s81, v100\n"
    "v_mfma_f32_16x16x32_bf16 v[32:35], a[108:111], a[12:15], v[32:35]\n"
    "v_max_f32 v29, v29, v27\n"
    "v_exp_f32 v41, v41\n"
    "v_mfma_f32_16x16x32_bf16 v[36:39], a[112:115], a[0:3], v[96:99]\n"
    "s_mov_b32 s80, 327680\n"
    "s_add_u32 m0, %[m0w], 40960\n"
    "s_nop 0\n"
    "buffer_load_dwordx4 %[voffk], %[srdk], s80 offen nt lds\n"
    "v_fma_f32 v43, v43, s81, v100\n"
    "v_mov_b32 v27, v29\n"
    "v_exp_f32 v42, v42\n"
    "v_mfma_f32_16x16x32_bf16 v[36:39], a[116:119], a[4:7], v[36:39]\n"
    "v_permlane16_swap_b32 v29, v27\n"
    "v_cvt_pk_bf16_f32 v72, v40, v41\n"
    "v_mfma_f32_16x16x32_bf16 v[36:39], a[120:123], a[8:11], v[36:39]\n"
    "v_max_f32 v29, v29, v27\n"
    "v_fma_f32 v44, v44, s81, v100\n"
    "v_mfma_f32_16x16x32_bf16 v[36:39], a[124:127], a[12:15], v[36:39]\n"
    "v_mov_b32 v27, v29\n"
    "v_exp_f32 v43, v43\n"
    "; -- h10b\n"
    "v_mfma_f32_16x16x32_bf16 v[128:131], a[128:131], v[64:67], 0\n"
    "v_fma_f32 v45, v45, s81, v100\n"
    "v_permlane32_swap_b32 v29, v27\n"
    "v_exp_f32 v44, v44\n"
    "v_mfma_f32_16x16x32_bf16 v[132:135], a[132:135], v[64:67], 0\n"
    "v_max_f32 v29, v29, v27\n"
    "v_cvt_pk_bf16_f32 v73, v42, v43\n"
    "v_mfma_f32_16x16x32_bf16 v[136:139], a[136:139], v[64:67], 0\n"
    "v_mul_f32 v29, s81, v29\n"
    "v_fma_f32 v46, v46, s81, v100\n"
    "v_mfma_f32_16x16x32_bf16 v[140:143], a[140:143], v[64:67], 0\n"
    "v_cndmask_b32 %[m3], v29, %[m3], s[94:95]\n"
    "v_exp_f32 v45, v45\n"
    "v_mfma_f32_16x16x32_bf16 v[144:147], a[144:147], v[64:67], 0\n"
    "buffer_load_dwordx4 v124, %[srdk], s80 offen offset:1024 nt lds\n"
    "v_fma_f32 v47, v47, s81, v100\n"
    "v_sub_f32 v108, 0, %[m3]\n"
    "v_exp_f32 v46, v46\n"
    "v_mfma_f32_16x16x32_bf16 v[148:151], a[148:151], v[64:67], 0\n"
    "v_sub_f32 v109, 0, %[m3]\n"
    "v_cvt_pk_bf16_f32 v74, v44, v45\n"
    "v_mfma_f32_16x16x32_bf16 v[152:155], a[152:155], v[64:67], 0\n"
    "v_sub_f32 v110, 0, %[m3]\n"
    "v_exp_f32 v47, v47\n"
    "v_mfma_f32_16x16x32_bf16 v[156:159], a[156:159], v[64:67], 0\n"
    "v_sub_f32 v111, 0, %[m3]\n"
    "v_cvt_pk_bf16_f32 v75, v46, v47\n"
    "s_nop 15\n"
    "v_mfma_f32_16x16x32_bf16 v[40:43], a[96:99], a[16:19], v[100:103]\n"
    "s_waitcnt vmcnt(15)\n"
    "s_barrier\n"
    "; -- h11a\n"
    "v_mfma_f32_16x16x32_bf16 v[40:43], a[100:103], a[20:23], v[40:43]\n"
    "ds_read_b128 a[64:67], v112 offset:16384\n"
    "v_fma_f32 v48, v48, s81, v104\n"
    "v_exp_f32 v32, v32\n"
    "v_fma_f32 v49, v49, s81, v104\n"
    "v_mfma_f32_16x16x32_bf16 v[40:43], a[104:107], a[24:27], v[40:43]\n"
    "ds_read_b128 a[68:71], v113 offset:16384\n"
    "v_exp_f32 v48, v48\n"
    "v_exp_f32 v33, v33\n"
    "v_mfma_f32_16x16x32_bf16 v[40:43], a[108:111], a[28:31], v[40:43]\n"
    "ds_read_b128 a[72:75], v114 offset:16384\n"
    "v_fma_f32 v50, v50, s81, v104\n"
    "v_exp_f32 v34, v34\n"
    "v_mfma_f32_16x16x32_bf16 v[44:47], a[112:115], a[16:19], v[100:103]\n"
    "s_add_u32 m0, %[m0w], 98304\n"
    "s_nop 0\n"
    "buffer_load_dwordx4 %[voffv], %[srdv], s80 offen nt lds\n"
    "v_exp_f32 v49, v49\n"
    "v_fma_f32 v51, v51, s81, v104\n"
    "v_cvt_pk_bf16_f32 v68, v32, v33\n"
    "v_mfma_f32_16x16x32_bf16 v[44:47], a[116:119], a[20:23], v[44:47]\n"
    "ds_read_b128 a[76:79], v115 offset:16384\n"
    "v_exp_f32 v50, v50\n"
    "v_cvt_pk_bf16_f32 v80, v48, v49\n"
    "v_mfma_f32_16x16x32_bf16 v[44:47], a[120:123], a[24:27], v[44:47]\n"
    "ds_read_b128 a[80:83], v112 offset:20480\n"
    "v_exp_f32 v35, v35\n"
    "v_fma_f32 v52, v52, s81, v104\n"
    "v_mfma_f32_16x16x32_bf16 v[44:47], a[124:127], a[28:31], v[44:47]\n"
    "ds_read_b128 a[84:87], v113 offset:20480\n"
    "v_exp_f32 v36, v36\n"
    "v_exp_f32 v51, v51\n"
    "; -- h11b\n"
    "v_mfma_f32_16x16x32_bf16 v[160:163], a[128:131], v[72:75], 0\n"
    "ds_read_b128 a[88:91], v114 offset:20480\n"
    "v_fma_f32 v53, v53, s81, v104\n"
    "v_cvt_pk_bf16_f32 v69, v34, v35\n"
    "v_mfma_f32_16x16x32_bf16 v[164:167], a[132:135], v[72:75], 0\n"
    "v_exp_f32 v52, v52\n"
    "v_cvt_pk_bf16_f32 v81, v50, v51\n"
    "v_exp_f32 v37, v37\n"
    "v_mfma_f32_16x16x32_bf16 v[168:171], a[136:139], v[72:75], 0\n"
    "ds_read_b128 a[92:95], v115 offset:20480\n"
    "v_fma_f32 v54, v54, s81, v104\n"
    "v_exp_f32 v38, v38\n"
    "v_mfma_f32_16x16x32_bf16 v[172:175], a[140:143], v[72:75], 0\n"
    "ds_read_b64_tr_b16 a[160:161], v116 offset:8192\n"
    "v_exp_f32 v53, v53\n"
    "v_mfma_f32_16x16x32_bf16 v[176:179], a[144:147], v[72:75], 0\n"
    "buffer_load_dwordx4 v125, %[srdv], s80 offen offset:1024 nt lds\n"
    "ds_read_b64_tr_b16 a[162:163], v116 offset:12288\n"
    "v_fma_f32 v55, v55, s81, v104\n"
    "v_cvt_pk_bf16_f32 v70, v36, v37\n"
    "v_mfma_f32_16x16x32_bf16 v[180:183], a[148:151], v[72:75], 0\n"
    "v_exp_f32 v54, v54\n"
    "v_cvt_pk_bf16_f32 v82, v52, v53\n"
    "v_exp_f32 v39, v39\n"
    "v_mfma_f32_16x16x32_bf16 v[184:187], a[152:155], v[72:75], 0\n"
    "ds_read_b64_tr_b16 a[164:165], v117 offset:8192\n"
    "v_exp_f32 v55, v55\n"
    "v_cvt_pk_bf16_f32 v71, v38, v39\n"
    "v_mfma_f32_16x16x32_bf16 v[188:191], a[156:159], v[72:75], 0\n"
    "ds_read_b64_tr_b16 a[166:167], v117 offset:12288\n"
    "v_cvt_pk_bf16_f32 v83, v54, v55\n"
    "s_nop 15\n"
    "; -- h12a\n"
    "v_mfma_f32_16x16x32_bf16 v[48:51], a[96:99], a[32:35], 0\n"
    "ds_read_b64_tr_b16 a[168:169], v118 offset:8192\n"
    "v_fma_f32 v56, v56, s81, v108\n"
    "v_exp_f32 v40, v40\n"
    "v_mfma_f32_16x16x32_bf16 v[48:51], a[100:103], a[36:39], v[48:51]\n"
    "v_fma_f32 v57, v57, s81, v108\n"
    "v_exp_f32 v56, v56\n"
    "v_exp_f32 v41, v41\n"
    "v_mfma_f32_16x16x32_bf16 v[48:51], a[104:107], a[40:43], v[48:51]\n"
    "ds_read_b64_tr_b16 a[170:171], v118 offset:12288\n"
    "v_fma_f32 v58, v58, s81, v108\n"
    "v_exp_f32 v42, v42\n"
    "v_mfma_f32_16x16x32_bf16 v[48:51], a[108:111], a[44:47], v[48:51]\n"
    "ds_read_b64_tr_b16 a[172:173], v119 offset:8192\n"
    "v_exp_f32 v57, v57\n"
    "v_mfma_f32_16x16x32_bf16 v[52:55], a[112:115], a[32:35], 0\n"
    "s_mov_b32 s80, 393216\n"
    "s_add_u32 m0, %[m0w], 49152\n"
    "s_nop 0\n"
    "buffer_load_dwordx4 %[voffk], %[srdk], s80 offen nt lds\n"
    "ds_read_b64_tr_b16 a[174:175], v119 offset:12288\n"
    "v_fma_f32 v59, v59, s81, v108\n"
    "v_cvt_pk_bf16_f32 v76, v40, v41\n"
    "v_mfma_f32_16x16x32_bf16 v[52:55], a[116:119], a[36:39], v[52:55]\n"
    "v_exp_f32 v58, v58\n"
    "v_cvt_pk_bf16_f32 v88, v56, v57\n"
    "v_exp_f32 v43, v43\n"
    "v_mfma_f32_16x16x32_bf16 v[52:55], a[120:123], a[40:43], v[52:55]\n"
    "ds_read_b64_tr_b16 a[176:177], v120 offset:8192\n"
    "v_fma_f32 v60, v60, s81, v108\n"
    "v_exp_f32 v44, v44\n"
    "v_mfma_f32_16x16x32_bf16 v[52:55], a[124:127], a[44:47], v[52:55]\n"
    "ds_read_b64_tr_b16 a[178:179], v120 offset:12288\n"
    "v_exp_f32 v59, v59\n"
    "; -- h12b\n"
    "v_mfma_f32_16x16x32_bf16 v[192:195], a[128:131], v[80:83], 0\n"
    "ds_read_b64_tr_b16 a[180:181], v121 offset:8192\n"
    "v_fma_f32 v61, v61, s81, v108\n"
    "v_accvgpr_read_b32 v21, a32\n"
    "v_cvt_pk_bf16_f32 v77, v42, v43\n"
    "v_accvgpr_read_b32 v23, a33\n"
    "v_mfma_f32_16x16x32_bf16 v[196:199], a[132:135], v[80:83], 0\n"
    "v_lshlrev_b32 v20, 16, v21\n"
    "v_exp_f32 v60, v60\n"
    "v_cvt_pk_bf16_f32 v89, v58, v59\n"
    "v_lshlrev_b32 v22, 16, v23\n"
    "v_exp_f32 v45, v45\n"
    "v_mfma_f32_16x16x32_bf16 v[200:203], a[136:139], v[80:83], 0\n"
    "ds_read_b64_tr_b16 a[182:183], v121 offset:12288\n"
    "v_and_b32 v21, 0xffff0000, v21\n"
    "v_fma_f32 v62, v62, s81, v108\n"
    "v_and_b32 v23, 0xffff0000, v23\n"
    "v_exp_f32 v46, v46\n"
    "v_mfma_f32_16x16x32_bf16 v[204:207], a[140:143], v[80:83], 0\n"
    "ds_read_b64_tr_b16 a[184:185], v122 offset:8192\n"
    "v_mul_f32 v20, s81, v20\n"
    "v_mul_f32 v22, s81, v22\n"
    "v_exp_f32 v61, v61\n"
    "v_mfma_f32_16x16x32_bf16 v[208:211], a[144:147], v[80:83], 0\n"
    "buffer_load_dwordx4 v124, %[srdk], s80 offen offset:1024 nt lds\n"
    "ds_read_b64_tr_b16 a[186:187], v122 offset:12288\n"
    "v_fma_f32 v63, v63, s81, v108\n"
    "v_mul_f32 v21, s81, v21\n"
    "v_cvt_pk_bf16_f32 v78, v44, v45\n"
    "v_mul_f32 v23, s81, v23\n"
    "v_mfma_f32_16x16x32_bf16 v[212:215], a[148:151], v[80:83], 0\n"
    "v_cvt_pk_bf16_f32 v20, v20, v21\n"
    "v_exp_f32 v62, v62\n"
    "v_cvt_pk_bf16_f32 v90, v60, v61\n"
    "v_cvt_pk_bf16_f32 v22, v22, v23\n"
    "v_exp_f32 v47, v47\n"
    "v_mfma_f32_16x16x32_bf16 v[216:219], a[152:155], v[80:83], 0\n"
    "ds_read_b64_tr_b16 a[188:189], v123 offset:8192\n"
    "v_accvgpr_write_b32 a32, v20\n"
    "v_exp_f32 v63, v63\n"
    "v_accvgpr_write_b32 a33, v22\n"
    "v_cvt_pk_bf16_f32 v79, v46, v47\n"
    "v_mfma_f32_16x16x32_bf16 v[220:223], a[156:159], v[80:83], 0\n"
    "ds_read_b64_tr_b16 a[190:191], v123 offset:12288\n"
    "v_accvgpr_read_b32 v21, a34\n"
    "v_accvgpr_read_b32 v23, a35\n"
    "v_cvt_pk_bf16_f32 v91, v62, v63\n"
    "s_nop 15\n"
    "; -- h13a\n"
    "v_mfma_f32_16x16x32_bf16 v[56:59], a[96:99], a[48:51], 0\n"
    "v_lshlrev_b32 v20, 16, v21\n"
    "v_fma_f32 v48, v48, s81, v104\n"
    "v_lshlrev_b32 v22, 16, v23\n"
    "v_fma_f32 v49, v49, s81, v104\n"
    "v_mfma_f32_16x16x32_bf16 v[56:59], a[100:103], a[52:55], v[56:59]\n"
    "v_and_b32 v21, 0xffff0000, v21\n"
    "v_and_b32 v23, 0xffff0000, v23\n"
    "v_exp_f32 v48, v48\n"
    "v_mfma_f32_16x16x32_bf16 v[56:59], a[104:107], a[56:59], v[56:59]\n"
    "v_mul_f32 v20, s81, v20\n"
    "v_mul_f32 v22, s81, v22\n"
    "v_fma_f32 v50, v50, s81, v104\n"
    "v_mfma_f32_16x16x32_bf16 v[56:59], a[108:111], a[60:63], v[56:59]\n"
    "v_mul_f32 v21, s81, v21\n"
    "v_exp_f32 v49, v49\n"
    "v_mul_f32 v23, s81, v23\n"
    "v_mfma_f32_16x16x32_bf16 v[60:63], a[112:115], a[48:51], 0\n"
    "s_add_u32 m0, %[m0w], 106496\n"
    "s_nop 0\n"
    "buffer_load_dwordx4 %[voffv], %[srdv], s80 offen nt lds\n"
    "v_cvt_pk_bf16_f32 v20, v20, v21\n"
    "v_fma_f32 v51, v51, s81, v104\n"
    "v_cvt_pk_bf16_f32 v22, v22, v23\n"
    "v_exp_f32 v50, v50\n"
    "v_mfma_f32_16x16x32_bf16 v[60:63], a[116:119], a[52:55], v[60:63]\n"
    "v_accvgpr_write_b32 a34, v20\n"
    "v_accvgpr_write_b32 a35, v22\n"
    "v_cvt_pk_bf16_f32 v84, v48, v49\n"
    "v_mfma_f32_16x16x32_bf16 v[60:63], a[120:123], a[56:59], v[60:63]\n"
    "v_accvgpr_read_b32 v21, a36\n"
    "v_accvgpr_read_b32 v23, a37\n"
    "v_fma_f32 v52, v52, s81, v104\n"
    "v_mfma_f32_16x16x32_bf16 v[60:63], a[124:127], a[60:63], v[60:63]\n"
    "v_lshlrev_b32 v20, 16, v21\n"
    "v_exp_f32 v51, v51\n"
    "v_lshlrev_b32 v22, 16, v23\n"
    "; -- h13b\n"
    "v_mfma_f32_16x16x32_bf16 v[224:227], a[128:131], v[88:91], 0\n"
    "v_and_b32 v21, 0xffff0000, v21\n"
    "v_accvgpr_read_b32 v25, a48\n"
    "v_fma_f32 v53, v53, s81, v104\n"
    "v_and_b32 v23, 0xffff0000, v23\n"
    "v_accvgpr_read_b32 v27, a49\n"
    "v_exp_f32 v52, v52\n"
    "v_mfma_f32_16x16x32_bf16 v[228:231], a[132:135], v[88:91], 0\n"
    "v_mul_f32 v20, s81, v20\n"
    "v_lshlrev_b32 v24, 16, v25\n"
    "v_mul_f32 v22, s81, v22\n"
    "v_lshlrev_b32 v26, 16, v27\n"
    "v_cvt_pk_bf16_f32 v85, v50, v51\n"
    "v_mfma_f32_16x16x32_bf16 v[232:235], a[136:139], v[88:91], 0\n"
    "v_mul_f32 v21, s81, v21\n"
    "v_and_b32 v25, 0xffff0000, v25\n"
    "v_mul_f32 v23, s81, v23\n"
    "v_and_b32 v27, 0xffff0000, v27\n"
    "v_fma_f32 v54, v54, s81, v104\n"
    "v_mfma_f32_16x16x32_bf16 v[236:239], a[140:143], v[88:91], 0\n"
    "v_cvt_pk_bf16_f32 v20, v20, v21\n"
    "v_mul_f32 v24, s81, v24\n"
    "v_exp_f32 v53, v53\n"
    "v_cvt_pk_bf16_f32 v22, v22, v23\n"
    "v_mul_f32 v26, s81, v26\n"
    "v_mfma_f32_16x16x32_bf16 v[240:243], a[144:147], v[88:91], 0\n"
    "buffer_load_dwordx4 v125, %[srdv], s80 offen offset:1024 nt lds\n"
    "v_accvgpr_write_b32 a36, v20\n"
    "v_mul_f32 v25, s81, v25\n"
    "v_fma_f32 v55, v55, s81, v104\n"
    "v_accvgpr_write_b32 a37, v22\n"
    "v_mul_f32 v27, s81, v27\n"
    "v_exp_f32 v54, v54\n"
    "v_mfma_f32_16x16x32_bf16 v[244:247], a[148:151], v[88:91], 0\n"
    "v_accvgpr_read_b32 v21, a38\n"
    "v_cvt_pk_bf16_f32 v24, v24, v25\n"
    "v_accvgpr_read_b32 v23, a39\n"
    "v_cvt_pk_bf16_f32 v26, v26, v27\n"
    "v_cvt_pk_bf16_f32 v86, v52, v53\n"
    "v_mfma_f32_16x16x32_bf16 v[248:251], a[152:155], v[88:91], 0\n"
    "v_lshlrev_b32 v20, 16, v21\n"
    "v_accvgpr_write_b32 a48, v24\n"
    "v_lshlrev_b32 v22, 16, v23\n"
    "v_accvgpr_write_b32 a49, v26\n"
    "v_exp_f32 v55, v55\n"
    "v_mfma_f32_16x16x32_bf16 v[252:255], a[156:159], v[88:91], 0\n"
    "v_and_b32 v21, 0xffff0000, v21\n"
    "v_accvgpr_read_b32 v25, a50\n"
    "v_cvt_pk_bf16_f32 v87, v54, v55\n"
    "v_and_b32 v23, 0xffff0000, v23\n"
    "v_accvgpr_read_b32 v27, a51\n"
    "s_nop 15\n"
    "; ---- head unit 2\n"
    "s_waitcnt lgkmcnt(0)\n"
    "; -- h20a\n"
    "v_mfma_f32_16x16x32_bf16 v[32:35], a[64:67], a[0:3], v[96:99]\n"
    "v_mul_f32 v20, s81, v20\n"
    "v_lshlrev_b32 v24, 16, v25\n"
    "v_fma_f32 v56, v56, s81, v108\n"
    "v_mul_f32 v22, s81, v22\n"
    "v_lshlrev_b32 v26, 16, v27\n"
    "v_fma_f32 v57, v57, s81, v108\n"
    "v_mfma_f32_16x16x32_bf16 v[32:35], a[68:71], a[4:7], v[32:35]\n"
    "v_mul_f32 v21, s81, v21\n"
    "v_and_b32 v25, 0xffff0000, v25\n"
    "v_mul_f32 v23, s81, v23\n"
    "v_and_b32 v27, 0xffff0000, v27\n"
    "v_exp_f32 v56, v56\n"
    "v_mfma_f32_16x16x32_bf16 v[32:35], a[72:75], a[8:11], v[32:35]\n"
    "v_cvt_pk_bf16_f32 v20, v20, v21\n"
    "v_mul_f32 v24, s81, v24\n"
    "v_cvt_pk_bf16_f32 v22, v22, v23\n"
    "v_mul_f32 v26, s81, v26\n"
    "v_fma_f32 v58, v58, s81, v108\n"
    "v_mfma_f32_16x16x32_bf16 v[32:35], a[76:79], a[12:15], v[32:35]\n"
    "v_accvgpr_write_b32 a38, v20\n"
    "v_mul_f32 v25, s81, v25\n"
    "v_exp_f32 v57, v57\n"
    "v_accvgpr_write_b32 a39, v22\n"
    "v_mul_f32 v27, s81, v27\n"
    "v_mfma_f32_16x16x32_bf16 v[36:39], a[80:83], a[0:3], v[96:99]\n"
    "s_mov_b32 s80, 458752\n"
    "s_add_u32 m0, %[m0w], 57344\n"
    "s_nop 0\n"
    "buffer_load_dwordx4 %[voffk], %[srdk], s80 offen nt lds\n"
    "v_accvgpr_read_b32 v21, a40\n"
    "v_cvt_pk_bf16_f32 v24, v24, v25\n"
    "v_fma_f32 v59, v59, s81, v108\n"
    "v_accvgpr_read_b32 v23, a41\n"
    "v_cvt_pk_bf16_f32 v26, v26, v27\n"
    "v_exp_f32 v58, v58\n"
    "v_mfma_f32_16x16x32_bf16 v[36:39], a[84:87], a[4:7], v[36:39]\n"
    "v_lshlrev_b32 v20, 16, v21\n"
    "v_accvgpr_write_b32 a50, v24\n"
    "v_lshlrev_b32 v22, 16, v23\n"
    "v_accvgpr_write_b32 a51, v26\n"
    "v_cvt_pk_bf16_f32 v92, v56, v57\n"
    "v_mfma_f32_16x16x32_bf16 v[36:39], a[88:91], a[8:11], v[36:39]\n"
    "v_and_b32 v21, 0xffff0000, v21\n"
    "v_accvgpr_read_b32 v25, a52\n"
    "v_and_b32 v23, 0xffff0000, v23\n"
    "v_accvgpr_read_b32 v27, a53\n"
    "v_fma_f32 v60, v60, s81, v108\n"
    "v_mfma_f32_16x16x32_bf16 v[36:39], a[92:95], a[12:15], v[36:39]\n"
    "v_mul_f32 v20, s81, v20\n"
    "v_lshlrev_b32 v24, 16, v25\n"
    "v_exp_f32 v59, v59\n"
    "v_mul_f32 v22, s81, v22\n"
    "v_lshlrev_b32 v26, 16, v27\n"
    "; -- h20b\n"
    "v_mfma_f32_16x16x32_bf16 v[128:131], a[160:163], v[68:71], v[128:131]\n"
    "v_mul_f32 v21, s81, v21\n"
    "v_and_b32 v25, 0xffff0000, v25\n"
    "v_fma_f32 v61, v61, s81, v108\n"
    "v_mul_f32 v23, s81, v23\n"
    "v_and_b32 v27, 0xffff0000, v27\n"
    "v_mfma_f32_16x16x32_bf16 v[132:135], a[164:167], v[68:71], v[132:135]\n"
    "v_exp_f32 v60, v60\n"
    "v_cvt_pk_bf16_f32 v20, v20, v21\n"
    "v_mul_f32 v24, s81, v24\n"
    "v_cvt_pk_bf16_f32 v22, v22, v23\n"
    "v_mul_f32 v26, s81, v26\n"
    "v_mfma_f32_16x16x32_bf16 v[136:139], a[168:171], v[68:71], v[136:139]\n"
    "v_cvt_pk_bf16_f32 v93, v58, v59\n"
    "v_accvgpr_write_b32 a40, v20\n"
    "v_mul_f32 v25, s81, v25\n"
    "v_accvgpr_write_b32 a41, v22\n"
    "v_mfma_f32_16x16x32_bf16 v[140:143], a[172:175], v[68:71], v[140:143]\n"
    "v_mul_f32 v27, s81, v27\n"
    "v_fma_f32 v62, v62, s81, v108\n"
    "v_accvgpr_read_b32 v21, a42\n"
    "v_cvt_pk_bf16_f32 v24, v24, v25\n"
    "v_exp_f32 v61, v61\n"
    "v_mfma_f32_16x16x32_bf16 v[144:147], a[176:179], v[68:71], v[144:147]\n"
    "buffer_load_dwordx4 v124, %[srdk], s80 offen offset:1024 nt lds\n"
    "v_accvgpr_read_b32 v23, a43\n"
    "v_cvt_pk_bf16_f32 v26, v26, v27\n"
    "v_lshlrev_b32 v20, 16, v21\n"
    "v_accvgpr_write_b32 a52, v24\n"
    "v_fma_f32 v63, v63, s81, v108\n"
    "v_mfma_f32_16x16x32_bf16 v[148:151], a[180:183], v[68:71], v[148:151]\n"
    "v_lshlrev_b32 v22, 16, v23\n"
    "v_accvgpr_write_b32 a53, v26\n"
    "v_exp_f32 v62, v62\n"
    "v_and_b32 v21, 0xffff0000, v21\n"
    "v_mfma_f32_16x16x32_bf16 v[152:155], a[184:187], v[68:71], v[152:155]\n"
    "v_accvgpr_read_b32 v25, a54\n"
    "v_and_b32 v23, 0xffff0000, v23\n"
    "v_accvgpr_read_b32 v27, a55\n"
    "v_cvt_pk_bf16_f32 v94, v60, v61\n"
    "v_mul_f32 v20, s81, v20\n"
    "v_mfma_f32_16x16x32_bf16 v[156:159], a[188:191], v[68:71], v[156:159]\n"
    "v_lshlrev_b32 v24, 16, v25\n"
    "v_mul_f32 v22, s81, v22\n"
    "v_lshlrev_b32 v26, 16, v27\n"
    "v_exp_f32 v63, v63\n"
    "v_mul_f32 v21, s81, v21\n"
    "v_smfmac_f32_16x16x64_bf16 a[192:195], a[208:211], v[64:71], v126\n"
    "v_and_b32 v25, 0xffff0000, v25\n"
    "v_cvt_pk_bf16_f32 v95, v62, v63\n"
    "v_mul_f32 v23, s81, v23\n"
    "v_and_b32 v27, 0xffff0000, v27\n"
    "s_nop 7\n"
    "v_mfma_f32_16x16x32_bf16 v[40:43], a[64:67], a[16:19], v[100:103]\n"
    "s_waitcnt vmcnt(10)\n"
    "s_barrier\n"
    "; -- h21a\n"
    "v_mfma_f32_16x16x32_bf16 v[40:43], a[68:71], a[20:23], v[40:43]\n"
    "ds_read_b128 a[96:99], v112 offset:24576\n"
    "v_cvt_pk_bf16_f32 v20, v20, v21\n"
    "v_mul_f32 v24, s81, v24\n"
    "v_exp_f32 v32, v32\n"
    "v_cvt_pk_bf16_f32 v22, v22, v23\n"
    "v_mul_f32 v26, s81, v26\n"
    "v_accvgpr_write_b32 a42, v20\n"
    "v_mfma_f32_16x16x32_bf16 v[40:43], a[72:75], a[24:27], v[40:43]\n"
    "v_mul_f32 v25, s81, v25\n"
    "v_exp_f32 v33, v33\n"
    "v_accvgpr_write_b32 a43, v22\n"
    "v_mul_f32 v27, s81, v27\n"
    "v_accvgpr_read_b32 v21, a44\n"
    "v_cvt_pk_bf16_f32 v24, v24, v25\n"
    "v_mfma_f32_16x16x32_bf16 v[40:43], a[76:79], a[28:31], v[40:43]\n"
    "ds_read_b128 a[100:103], v113 offset:24576\n"
    "v_accvgpr_read_b32 v23, a45\n"
    "v_cvt_pk_bf16_f32 v26, v26, v27\n"
    "v_exp_f32 v34, v34\n"
    "v_lshlrev_b32 v20, 16, v21\n"
    "v_accvgpr_write_b32 a54, v24\n"
    "v_mfma_f32_16x16x32_bf16 v[44:47], a[80:83], a[16:19], v[100:103]\n"
    "s_add_u32 m0, %[m0w], 114688\n"
    "s_nop 0\n"
    "buffer_load_dwordx4 %[voffv], %[srdv], s80 offen nt lds\n"
    "ds_read_b128 a[104:107], v114 offset:24576\n"
    "v_lshlrev_b32 v22, 16, v23\n"
    "v_accvgpr_write_b32 a55, v26\n"
    "v_and_b32 v21, 0xffff0000, v21\n"
    "v_accvgpr_read_b32 v25, a56\n"
    "v_cvt_pk_bf16_f32 v64, v32, v33\n"
    "v_and_b32 v23, 0xffff0000, v23\n"
    "v_mfma_f32_16x16x32_bf16 v[44:47], a[84:87], a[20:23], v[44:47]\n"
    "ds_read_b128 a[108:111], v115 offset:24576\n"
    "v_accvgpr_read_b32 v27, a57\n"
    "v_mul_f32 v20, s81, v20\n"
    "v_lshlrev_b32 v24, 16, v25\n"
    "v_exp_f32 v35, v35\n"
    "v_mul_f32 v22, s81, v22\n"
    "v_mfma_f32_16x16x32_bf16 v[44:47], a[88:91], a[24:27], v[44:47]\n"
    "ds_read_b128 a[112:115], v112 offset:28672\n"
    "v_lshlrev_b32 v26, 16, v27\n"
    "v_mul_f32 v21, s81, v21\n"
    "v_and_b32 v25, 0xffff0000, v25\n"
    "v_mul_f32 v23, s81, v23\n"
    "v_and_b32 v27, 0xffff0000, v27\n"
    "v_mfma_f32_16x16x32_bf16 v[44:47], a[92:95], a[28:31], v[44:47]\n"
    "ds_read_b128 a[116:119], v113 offset:28672\n"
    "v_exp_f32 v36, v36\n"
    "v_cvt_pk_bf16_f32 v20, v20, v21\n"
    "v_mul_f32 v24, s81, v24\n"
    "v_cvt_pk_bf16_f32 v22, v22, v23\n"
    "v_mul_f32 v26, s81, v26\n"
    "; -- h21b\n"
    "v_mfma_f32_16x16x32_bf16 v[160:163], a[160:163], v[76:79], v[160:163]\n"
    "v_accvgpr_write_b32 a44, v20\n"
    "v_mul_f32 v25, s81, v25\n"
    "v_cvt_pk_bf16_f32 v65, v34, v35\n"
    "v_accvgpr_write_b32 a45, v22\n"
    "v_mul_f32 v27, s81, v27\n"
    "v_mfma_f32_16x16x32_bf16 v[164:167], a[164:167], v[76:79], v[164:167]\n"
    "ds_read_b128 a[120:123], v114 offset:28672\n"
    "v_accvgpr_read_b32 v21, a46\n"
    "v_cvt_pk_bf16_f32 v24, v24, v25\n"
    "v_exp_f32 v37, v37\n"
    "v_accvgpr_read_b32 v23, a47\n"
    "v_mfma_f32_16x16x32_bf16 v[168:171], a[168:171], v[76:79], v[168:171]\n"
    "ds_read_b128 a[124:127], v115 offset:28672\n"
    "v_cvt_pk_bf16_f32 v26, v26, v27\n"
    "v_lshlrev_b32 v20, 16, v21\n"
    "v_accvgpr_write_b32 a56, v24\n"
    "v_lshlrev_b32 v22, 16, v23\n"
    "v_mfma_f32_16x16x32_bf16 v[172:175], a[172:175], v[76:79], v[172:175]\n"
    "ds_read_b64_tr_b16 a[128:129], v116 offset:16384\n"
    "v_accvgpr_write_b32 a57, v26\n"
    "v_exp_f32 v38, v38\n"
    "v_and_b32 v21, 0xffff0000, v21\n"
    "v_accvgpr_read_b32 v25, a58\n"
    "v_mfma_f32_16x16x32_bf16 v[176:179], a[176:179], v[76:79], v[176:179]\n"
    "buffer_load_dwordx4 v125, %[srdv], s80 offen offset:1024 nt lds\n"
    "v_and_b32 v23, 0xffff0000, v23\n"
    "v_accvgpr_read_b32 v27, a59\n"
    "v_mul_f32 v20, s81, v20\n"
    "v_lshlrev_b32 v24, 16, v25\n"
    "v_cvt_pk_bf16_f32 v66, v36, v37\n"
    "v_mfma_f32_16x16x32_bf16 v[180:183], a[180:183], v[76:79], v[180:183]\n"
    "ds_read_b64_tr_b16 a[130:131], v116 offset:20480\n"
    "v_mul_f32 v22, s81, v22\n"
    "v_lshlrev_b32 v26, 16, v27\n"
    "v_mul_f32 v21, s81, v21\n"
    "v_and_b32 v25, 0xffff0000, v25\n"
    "v_mfma_f32_16x16x32_bf16 v[184:187], a[184:187], v[76:79], v[184:187]\n"
    "v_exp_f32 v39, v39\n"
    "v_mul_f32 v23, s81, v23\n"
    "v_and_b32 v27, 0xffff0000, v27\n"
    "v_cvt_pk_bf16_f32 v20, v20, v21\n"
    "v_mul_f32 v24, s81, v24\n"
    "v_mfma_f32_16x16x32_bf16 v[188:191], a[188:191], v[76:79], v[188:191]\n"
    "ds_read_b64_tr_b16 a[132:133], v117 offset:16384\n"
    "v_cvt_pk_bf16_f32 v22, v22, v23\n"
    "v_mul_f32 v26, s81, v26\n"
    "v_cvt_pk_bf16_f32 v67, v38, v39\n"
    "v_accvgpr_write_b32 a46, v20\n"
    "v_smfmac_f32_16x16x64_bf16 a[196:199], a[208:211], v[72:79], v126\n"
    "ds_read_b64_tr_b16 a[134:135], v117 offset:20480\n"
    "v_mul_f32 v25, s81, v25\n"
    "v_accvgpr_write_b32 a47, v22\n"
    "v_mul_f32 v27, s81, v27\n"
    "s_nop 7\n"
    "buffer_load_dwordx2 a[212:213], off, %[srdw], %[epo] sc1\n"
    "s_add_u32 s82, %[sbase], 68608\n"
    "buffer_load_dword a254, off, %[srdw], %[sbase] offset:256 sc1\n"
    "buffer_load_dword a255, off, %[srdw], s82 offset:256 sc1\n"
    "; -- h22a\n"
    "v_mfma_f32_16x16x32_bf16 v[48:51], a[64:67], a[32:35], v[104:107]\n"
    "ds_read_b64_tr_b16 a[136:137], v118 offset:16384\n"
    "v_cvt_pk_bf16_f32 v24, v24, v25\n"
    "v_exp_f32 v40, v40\n"
    "v_cvt_pk_bf16_f32 v26, v26, v27\n"
    "v_mfma_f32_16x16x32_bf16 v[48:51], a[68:71], a[36:39], v[48:51]\n"
    "v_accvgpr_write_b32 a58, v24\n"
    "v_exp_f32 v41, v41\n"
    "v_accvgpr_write_b32 a59, v26\n"
    "v_mfma_f32_16x16x32_bf16 v[48:51], a[72:75], a[40:43], v[48:51]\n"
    "ds_read_b64_tr_b16 a[138:139], v118 offset:20480\n"
    "v_accvgpr_read_b32 v25, a60\n"
    "v_accvgpr_read_b32 v27, a61\n"
    "v_exp_f32 v42, v42\n"
    "v_mfma_f32_16x16x32_bf16 v[48:51], a[76:79], a[44:47], v[48:51]\n"
    "ds_read_b64_tr_b16 a[140:141], v119 offset:16384\n"
    "v_lshlrev_b32 v24, 16, v25\n"
    "v_lshlrev_b32 v26, 16, v27\n"
    "v_mfma_f32_16x16x32_bf16 v[52:55], a[80:83], a[32:35], v[104:107]\n"
    "s_mov_b32 s80, 524288\n"
    "s_add_u32 m0, %[m0w], 0\n"
    "s_nop 0\n"
    "buffer_load_dwordx4 %[voffk], %[srdk], s80 offen nt lds\n"
    "ds_read_b64_tr_b16 a[142:143], v119 offset:20480\n"
    "v_and_b32 v25, 0xffff0000, v25\n"
    "v_cvt_pk_bf16_f32 v72, v40, v41\n"
    "v_and_b32 v27, 0xffff0000, v27\n"
    "v_mfma_f32_16x16x32_bf16 v[52:55], a[84:87], a[36:39], v[52:55]\n"
    "v_mul_f32 v24, s81, v24\n"
    "v_exp_f32 v43, v43\n"
    "v_mul_f32 v26, s81, v26\n"
    "v_mfma_f32_16x16x32_bf16 v[52:55], a[88:91], a[40:43], v[52:55]\n"
    "ds_read_b64_tr_b16 a[144:145], v120 offset:16384\n"
    "v_mul_f32 v25, s81, v25\n"
    "v_mul_f32 v27, s81, v27\n"
    "v_exp_f32 v44, v44\n"
    "v_mfma_f32_16x16x32_bf16 v[52:55], a[92:95], a[44:47], v[52:55]\n"
    "ds_read_b64_tr_b16 a[146:147], v120 offset:20480\n"
    "v_cvt_pk_bf16_f32 v24, v24, v25\n"
    "v_cvt_pk_bf16_f32 v26, v26, v27\n"
    "; -- h22b\n"
    "v_mfma_f32_16x16x32_bf16 v[192:195], a[160:163], v[84:87], v[192:195]\n"
    "ds_read_b64_tr_b16 a[148:149], v121 offset:16384\n"
    "v_accvgpr_write_b32 a60, v24\n"
    "v_cvt_pk_bf16_f32 v73, v42, v43\n"
    "v_accvgpr_write_b32 a61, v26\n"
    "v_mfma_f32_16x16x32_bf16 v[196:199], a[164:167], v[84:87], v[196:199]\n"
    "v_accvgpr_read_b32 v25, a62\n"
    "v_exp_f32 v45, v45\n"
    "v_accvgpr_read_b32 v27, a63\n"
    "v_mfma_f32_16x16x32_bf16 v[200:203], a[168:171], v[84:87], v[200:203]\n"
    "ds_read_b64_tr_b16 a[150:151], v121 offset:20480\n"
    "v_lshlrev_b32 v24, 16, v25\n"
    "v_lshlrev_b32 v26, 16, v27\n"
    "v_mfma_f32_16x16x32_bf16 v[204:207], a[172:175], v[84:87], v[204:207]\n"
    "ds_read_b64_tr_b16 a[152:153], v122 offset:16384\n"
    "v_exp_f32 v46, v46\n"
    "v_and_b32 v25, 0xffff0000, v25\n"
    "v_mfma_f32_16x16x32_bf16 v[208:211], a[176:179], v[84:87], v[208:211]\n"
    "buffer_load_dwordx4 v124, %[srdk], s80 offen offset:1024 nt lds\n"
    "v_and_b32 v27, 0xffff0000, v27\n"
    "v_mul_f32 v24, s81, v24\n"
    "v_cvt_pk_bf16_f32 v74, v44, v45\n"
    "v_mfma_f32_16x16x32_bf16 v[212:215], a[180:183], v[84:87], v[212:215]\n"
    "ds_read_b64_tr_b16 a[154:155], v122 offset:20480\n"
    "v_mul_f32 v26, s81, v26\n"
    "v_mul_f32 v25, s81, v25\n"
    "v_mfma_f32_16x16x32_bf16 v[216:219], a[184:187], v[84:87], v[216:219]\n"
    "v_exp_f32 v47, v47\n"
    "v_mul_f32 v27, s81, v27\n"
    "v_cvt_pk_bf16_f32 v24, v24, v25\n"
    "v_mfma_f32_16x16x32_bf16 v[220:223], a[188:191], v[84:87], v[220:223]\n"
    "ds_read_b64_tr_b16 a[156:157], v123 offset:16384\n"
    "v_cvt_pk_bf16_f32 v26, v26, v27\n"
    "v_cvt_pk_bf16_f32 v75, v46, v47\n"
    "v_smfmac_f32_16x16x64_bf16 a[200:203], a[208:211], v[80:87], v126\n"
    "ds_read_b64_tr_b16 a[158:159], v123 offset:20480\n"
    "v_accvgpr_write_b32 a62, v24\n"
    "v_accvgpr_write_b32 a63, v26\n"
    "s_nop 7\n"
    "; -- h23a\n"
    "v_mfma_f32_16x16x32_bf16 v[56:59], a[64:67], a[48:51], v[108:111]\n"
    "v_exp_f32 v48, v48\n"
    "v_mfma_f32_16x16x32_bf16 v[56:59], a[68:71], a[52:55], v[56:59]\n"
    "Lh1_%=:\n"
    "s_cbranch_execz Lr1_%=\n"
    "v_exp_f32 v49, v49\n"
    "v_mfma_f32_16x16x32_bf16 v[56:59], a[72:75], a[56:59], v[56:59]\n"
    "v_exp_f32 v50, v50\n"
    "v_mfma_f32_16x16x32_bf16 v[56:59], a[76:79], a[60:63], v[56:59]\n"
    "v_mfma_f32_16x16x32_bf16 v[60:63], a[80:83], a[48:51], v[108:111]\n"
    "s_add_u32 m0, %[m0w], 122880\n"
    "s_nop 0\n"
    "buffer_load_dwordx4 %[voffv], %[srdv], s80 offen nt lds\n"
    "v_cvt_pk_bf16_f32 v80, v48, v49\n"
    "v_mfma_f32_16x16x32_bf16 v[60:63], a[84:87], a[52:55], v[60:63]\n"
    "v_exp_f32 v51, v51\n"
    "v_mfma_f32_16x16x32_bf16 v[60:63], a[88:91], a[56:59], v[60:63]\n"
    "v_exp_f32 v52, v52\n"
    "v_mfma_f32_16x16x32_bf16 v[60:63], a[92:95], a[60:63], v[60:63]\n"
    "; -- h23b\n"
    "v_mfma_f32_16x16x32_bf16 v[224:227], a[160:163], v[92:95], v[224:227]\n"
    "v_cvt_pk_bf16_f32 v81, v50, v51\n"
    "v_mfma_f32_16x16x32_bf16 v[228:231], a[164:167], v[92:95], v[228:231]\n"
    "v_exp_f32 v53, v53\n"
    "v_mfma_f32_16x16x32_bf16 v[232:235], a[168:171], v[92:95], v[232:235]\n"
    "v_mfma_f32_16x16x32_bf16 v[236:239], a[172:175], v[92:95], v[236:239]\n"
    "v_exp_f32 v54, v54\n"
    "v_mfma_f32_16x16x32_bf16 v[240:243], a[176:179], v[92:95], v[240:243]\n"
    "buffer_load_dwordx4 v125, %[srdv], s80 offen offset:1024 nt lds\n"
    "v_cvt_pk_bf16_f32 v82, v52, v53\n"
    "v_mfma_f32_16x16x32_bf16 v[244:247], a[180:183], v[92:95], v[244:247]\n"
    "v_mfma_f32_16x16x32_bf16 v[248:251], a[184:187], v[92:95], v[248:251]\n"
    "v_exp_f32 v55, v55\n"
    "v_mfma_f32_16x16x32_bf16 v[252:255], a[188:191], v[92:95], v[252:255]\n"
    "v_cvt_pk_bf16_f32 v83, v54, v55\n"
    "v_smfmac_f32_16x16x64_bf16 a[204:207], a[208:211], v[88:95], v126\n"
    "s_nop 7\n"
    "; ---- head unit 3\n"
    "s_waitcnt lgkmcnt(0)\n"
    "s_cmp_eq_u32 %[own], 0\n"
    "s_cbranch_scc1 Lhop%=\n"
    "s_cmp_eq_u32 %[own], 2\n"
    "s_cbranch_scc1 Lt2%=\n"
    "v_and_b32 v28, 0xf0, v127\n"
    "v_lshrrev_b32 v29, 5, v127\n"
    "v_add_u32 v20, 131072, v112\n"
    "v_add_u32 v21, 131072, v113\n"
    "v_add_u32 v22, 131072, v114\n"
    "v_add_u32 v23, 131072, v115\n"
    "v_and_b32 v29, 0x18, v29\n"
    "v_lshl_or_b32 v28, v28, 2, v29\n"
    "v_add_u32 v28, 147456, v28\n"
    "ds_read_b128 a[64:67], v20 offset:0\n"
    "ds_read_b128 a[68:71], v21 offset:0\n"
    "ds_read_b128 a[72:75], v22 offset:0\n"
    "ds_read_b128 a[76:79], v23 offset:0\n"
    "ds_read_b128 a[80:83], v20 offset:4096\n"
    "ds_read_b128 a[84:87], v21 offset:4096\n"
    "ds_read_b128 a[88:91], v22 offset:4096\n"
    "ds_read_b128 a[92:95], v23 offset:4096\n"
    "ds_read_b64 v[48:49], v28 offset:0\n"
    "ds_read_b64 v[50:51], v28 offset:32\n"
    "ds_read_b64 v[52:53], v28 offset:1024\n"
    "ds_read_b64 v[54:55], v28 offset:1056\n"
    "v_add_u32 v20, 73728, v116\n"
    "v_add_u32 v21, 73728, v117\n"
    "v_add_u32 v22, 73728, v118\n"
    "v_add_u32 v23, 73728, v119\n"
    "v_add_u32 v24, 73728, v120\n"
    "v_add_u32 v25, 73728, v121\n"
    "v_add_u32 v26, 73728, v122\n"
    "v_add_u32 v27, 73728, v123\n"
    "s_mov_b32 s83, 0xceabfb8f\n"
    "; -- t0\n"
    "s_waitcnt lgkmcnt(11)\n"
    "v_mfma_f32_16x16x32_bf16 v[32:35], a[64:67], a[0:3], v[96:99]\n"
    "ds_read_b64_tr_b16 a[160:161], v20 offset:0\n"
    "ds_read_b64_tr_b16 a[162:163], v20 offset:4096\n"
    "s_waitcnt lgkmcnt(12)\n"
    "v_mfma_f32_16x16x32_bf16 v[32:35], a[68:71], a[4:7], v[32:35]\n"
    "ds_read_b64_tr_b16 a[164:165], v21 offset:0\n"
    "ds_read_b64_tr_b16 a[166:167], v21 offset:4096\n"
    "s_waitcnt lgkmcnt(10)\n"
    "v_mfma_f32_16x16x32_bf16 v[32:35], a[72:75], a[8:11], v[32:35]\n"
    "ds_read_b64_tr_b16 a[168:169], v22 offset:0\n"
    "ds_read_b64_tr_b16 a[170:171], v22 offset:4096\n"
    "v_mfma_f32_16x16x32_bf16 v[32:35], a[76:79], a[12:15], v[32:35]\n"
    "ds_read_b64_tr_b16 a[172:173], v23 offset:0\n"
    "ds_read_b64_tr_b16 a[174:175], v23 offset:4096\n"
    "v_mfma_f32_16x16x32_bf16 v[36:39], a[80:83], a[0:3], v[96:99]\n"
    "ds_read_b64_tr_b16 a[176:177], v24 offset:0\n"
    "ds_read_b64_tr_b16 a[178:179], v24 offset:4096\n"
    "v_mfma_f32_16x16x32_bf16 v[36:39], a[84:87], a[4:7], v[36:39]\n"
    "ds_read_b64_tr_b16 a[180:181], v25 offset:0\n"
    "ds_read_b64_tr_b16 a[182:183], v25 offset:4096\n"
    "s_waitcnt lgkmcnt(12)\n"
    "v_mfma_f32_16x16x32_bf16 v[36:39], a[88:91], a[8:11], v[36:39]\n"
    "ds_read_b64_tr_b16 a[184:185], v26 offset:0\n"
    "ds_read_b64_tr_b16 a[186:187], v26 offset:4096\n"
    "v_mfma_f32_16x16x32_bf16 v[36:39], a[92:95], a[12:15], v[36:39]\n"
    "ds_read_b64_tr_b16 a[188:189], v27 offset:0\n"
    "ds_read_b64_tr_b16 a[190:191], v27 offset:4096\n"
    "; -- t1\n"
    "v_mfma_f32_16x16x32_bf16 v[40:43], a[64:67], a[16:19], v[100:103]\n"
    "v_lshlrev_b32 v20, 16, v48\n"
    "v_and_b32 v21, 0xffff0000, v48\n"
    "v_mfma_f32_16x16x32_bf16 v[40:43], a[68:71], a[20:23], v[40:43]\n"
    "v_lshlrev_b32 v22, 16, v49\n"
    "v_and_b32 v23, 0xffff0000, v49\n"
    "v_mfma_f32_16x16x32_bf16 v[40:43], a[72:75], a[24:27], v[40:43]\n"
    "v_lshlrev_b32 v24, 16, v50\n"
    "v_and_b32 v25, 0xffff0000, v50\n"
    "v_mfma_f32_16x16x32_bf16 v[40:43], a[76:79], a[28:31], v[40:43]\n"
    "v_lshlrev_b32 v26, 16, v51\n"
    "v_and_b32 v27, 0xffff0000, v51\n"
    "v_mfma_f32_16x16x32_bf16 v[44:47], a[80:83], a[16:19], v[100:103]\n"
    "v_fma_f32 v20, v20, -s83, s83\n"
    "v_fma_f32 v21, v21, -s83, s83\n"
    "v_mfma_f32_16x16x32_bf16 v[44:47], a[84:87], a[20:23], v[44:47]\n"
    "v_fma_f32 v22, v22, -s83, s83\n"
    "v_fma_f32 v23, v23, -s83, s83\n"
    "v_mfma_f32_16x16x32_bf16 v[44:47], a[88:91], a[24:27], v[44:47]\n"
    "v_fma_f32 v24, v24, -s83, s83\n"
    "v_fma_f32 v25, v25, -s83, s83\n"
    "v_mfma_f32_16x16x32_bf16 v[44:47], a[92:95], a[28:31], v[44:47]\n"
    "v_fma_f32 v26, v26, -s83, s83\n"
    "v_fma_f32 v27, v27, -s83, s83\n"
    "v_add_f32 v32, v32, v20\n"
    "v_add_f32 v33, v33, v21\n"
    "v_add_f32 v34, v34, v22\n"
    "v_add_f32 v35, v35, v23\n"
    "v_add_f32 v36, v36, v24\n"
    "v_add_f32 v37, v37, v25\n"
    "v_add_f32 v38, v38, v26\n"
    "v_add_f32 v39, v39, v27\n"
    "v_lshlrev_b32 v20, 16, v52\n"
    "v_exp_f32 v32, v32\n"
    "v_and_b32 v21, 0xffff0000, v52\n"
    "v_lshlrev_b32 v22, 16, v53\n"
    "v_exp_f32 v33, v33\n"
    "v_and_b32 v23, 0xffff0000, v53\n"
    "v_lshlrev_b32 v24, 16, v54\n"
    "v_exp_f32 v34, v34\n"
    "v_and_b32 v25, 0xffff0000, v54\n"
    "v_lshlrev_b32 v26, 16, v55\n"
    "v_exp_f32 v35, v35\n"
    "v_and_b32 v27, 0xffff0000, v55\n"
    "v_fma_f32 v20, v20, -s83, s83\n"
    "v_exp_f32 v36, v36\n"
    "v_fma_f32 v21, v21, -s83, s83\n"
    "v_fma_f32 v22, v22, -s83, s83\n"
    "v_exp_f32 v37, v37\n"
    "v_fma_f32 v23, v23, -s83, s83\n"
    "v_fma_f32 v24, v24, -s83, s83\n"
    "v_exp_f32 v38, v38\n"
    "v_fma_f32 v25, v25, -s83, s83\n"
    "v_fma_f32 v26, v26, -s83, s83\n"
    "v_exp_f32 v39, v39\n"
    "v_fma_f32 v27, v27, -s83, s83\n"
    "v_cvt_pk_bf16_f32 v68, v32, v33\n"
    "v_cvt_pk_bf16_f32 v69, v34, v35\n"
    "v_cvt_pk_bf16_f32 v70, v36, v37\n"
    "v_cvt_pk_bf16_f32 v71, v38, v39\n"
    "; -- t2\n"
    "s_waitcnt lgkmcnt(0)\n"
    "s_nop 0\n"
    "v_mfma_f32_16x16x32_bf16 v[128:131], a[160:163], v[68:71], v[128:131]\n"
    "v_add_f32 v40, v40, v20\n"
    "v_add_f32 v41, v41, v21\n"
    "v_mfma_f32_16x16x32_bf16 v[132:135], a[164:167], v[68:71], v[132:135]\n"
    "v_add_f32 v42, v42, v22\n"
    "v_add_f32 v43, v43, v23\n"
    "v_mfma_f32_16x16x32_bf16 v[136:139], a[168:171], v[68:71], v[136:139]\n"
    "v_add_f32 v44, v44, v24\n"
    "v_add_f32 v45, v45, v25\n"
    "v_mfma_f32_16x16x32_bf16 v[140:143], a[172:175], v[68:71], v[140:143]\n"
    "v_add_f32 v46, v46, v26\n"
    "v_add_f32 v47, v47, v27\n"
    "v_mfma_f32_16x16x32_bf16 v[144:147], a[176:179], v[68:71], v[144:147]\n"
    "v_exp_f32 v40, v40\n"
    "v_exp_f32 v41, v41\n"
    "v_mfma_f32_16x16x32_bf16 v[148:151], a[180:183], v[68:71], v[148:151]\n"
    "v_exp_f32 v42, v42\n"
    "v_exp_f32 v43, v43\n"
    "v_mfma_f32_16x16x32_bf16 v[152:155], a[184:187], v[68:71], v[152:155]\n"
    "v_exp_f32 v44, v44\n"
    "v_exp_f32 v45, v45\n"
    "v_mfma_f32_16x16x32_bf16 v[156:159], a[188:191], v[68:71], v[156:159]\n"
    "v_exp_f32 v46, v46\n"
    "v_exp_f32 v47, v47\n"
    "v_mov_b32 v20, 0x3f003f00\n"
    "v_mov_b32 v21, 0x3f003f00\n"
    "v_mov_b32 v22, 0x3f003f00\n"
    "v_mov_b32 v23, 0x3f003f00\n"
    "v_cvt_pk_bf16_f32 v76, v40, v41\n"
    "v_cvt_pk_bf16_f32 v77, v42, v43\n"
    "v_cvt_pk_bf16_f32 v78, v44, v45\n"
    "v_cvt_pk_bf16_f32 v79, v46, v47\n"
    "v_mfma_f32_16x16x32_bf16 a[192:195], v[20:23], v[68:71], a[192:195]\n"
    "s_nop 0\n"
    "v_mfma_f32_16x16x32_bf16 v[160:163], a[160:163], v[76:79], v[160:163]\n"
    "v_mfma_f32_16x16x32_bf16 v[164:167], a[164:167], v[76:79], v[164:167]\n"
    "v_mfma_f32_16x16x32_bf16 v[168:171], a[168:171], v[76:79], v[168:171]\n"
    "v_mfma_f32_16x16x32_bf16 v[172:175], a[172:175], v[76:79], v[172:175]\n"
    "v_mfma_f32_16x16x32_bf16 v[176:179], a[176:179], v[76:79], v[176:179]\n"
    "v_mfma_f32_16x16x32_bf16 v[180:183], a[180:183], v[76:79], v[180:183]\n"
    "v_mfma_f32_16x16x32_bf16 v[184:187], a[184:187], v[76:79], v[184:187]\n"
    "v_mfma_f32_16x16x32_bf16 v[188:191], a[188:191], v[76:79], v[188:191]\n"
    "v_mfma_f32_16x16x32_bf16 a[196:199], v[20:23], v[76:79], a[196:199]\n"
    "s_branch Ltskip%=\n"
    "Lhop%=:\n"
    "s_mov_b64 exec, 0\n"
    "s_bitcmp1_b32 %[m0w], 11\n"
    "s_cbranch_scc1 Lhop1%=\n"
    "s_branch Lh4_%=\n"
    "Lr4_%=:\n"
    "s_branch Lh6_%=\n"
    "Lr6_%=:\n"
    "s_branch Lh8_%=\n"
    "Lr8_%=:\n"
    "s_branch Lh10_%=\n"
    "Lr10_%=:\n"
    "s_branch Lh12_%=\n"
    "Lr12_%=:\n"
    "s_branch Lhop2%=\n"
    "Lhop1%=:\n"
    "s_branch Lh5_%=\n"
    "Lr5_%=:\n"
    "s_branch Lh7_%=\n"
    "Lr7_%=:\n"
    "s_branch Lh9_%=\n"
    "Lr9_%=:\n"
    "s_branch Lh11_%=\n"
    "Lr11_%=:\n"
    "s_branch Lh13_%=\n"
    "Lr13_%=:\n"
    "Lhop2%=:\n"
    "s_mov_b64 exec, -1\n"
    "s_branch Ltskip%=\n"
    "Lt2%=:\n"
    "Lh2_%=:\n"
    "s_cbranch_execz Lr2_%=\n"
    "v_and_b32 v28, 0xf0, v127\n"
    "v_lshrrev_b32 v29, 5, v127\n"
    "v_add_u32 v20, 131072, v112\n"
    "v_add_u32 v21, 131072, v113\n"
    "v_add_u32 v22, 131072, v114\n"
    "v_add_u32 v23, 131072, v115\n"
    "v_and_b32 v29, 0x18, v29\n"
    "v_lshl_or_b32 v28, v28, 2, v29\n"
    "v_add_u32 v28, 147456, v28\n"
    "ds_read_b128 a[64:67], v20 offset:0\n"
    "ds_read_b128 a[68:71], v21 offset:0\n"
    "ds_read_b128 a[72:75], v22 offset:0\n"
    "ds_read_b128 a[76:79], v23 offset:0\n"
    "ds_read_b128 a[80:83], v20 offset:4096\n"
    "ds_read_b128 a[84:87], v21 offset:4096\n"
    "ds_read_b128 a[88:91], v22 offset:4096\n"
    "ds_read_b128 a[92:95], v23 offset:4096\n"
    "ds_read_b64 v[48:49], v28 offset:0\n"
    "ds_read_b64 v[50:51], v28 offset:32\n"
    "ds_read_b64 v[52:53], v28 offset:1024\n"
    "ds_read_b64 v[54:55], v28 offset:1056\n"
    "v_add_u32 v20, 73728, v116\n"
    "v_add_u32 v21, 73728, v117\n"
    "v_add_u32 v22, 73728, v118\n"
    "v_add_u32 v23, 73728, v119\n"
    "v_add_u32 v24, 73728, v120\n"
    "v_add_u32 v25, 73728, v121\n"
    "v_add_u32 v26, 73728, v122\n"
    "v_add_u32 v27, 73728, v123\n"
    "s_mov_b32 s83, 0xceabfb8f\n"
    "; -- t0\n"
    "s_waitcnt lgkmcnt(11)\n"
    "v_mfma_f32_16x16x32_bf16 v[32:35], a[64:67], a[32:35], v[104:107]\n"
    "ds_read_b64_tr_b16 a[160:161], v20 offset:0\n"
    "ds_read_b64_tr_b16 a[162:163], v20 offset:4096\n"
    "s_waitcnt lgkmcnt(12)\n"
    "v_mfma_f32_16x16x32_bf16 v[32:35], a[68:71], a[36:39], v[32:35]\n"
    "ds_read_b64_tr_b16 a[164:165], v21 offset:0\n"
    "ds_read_b64_tr_b16 a[166:167], v21 offset:4096\n"
    "s_waitcnt lgkmcnt(10)\n"
    "v_mfma_f32_16x16x32_bf16 v[32:35], a[72:75], a[40:43], v[32:35]\n"
    "ds_read_b64_tr_b16 a[168:169], v22 offset:0\n"
    "ds_read_b64_tr_b16 a[170:171], v22 offset:4096\n"
    "v_mfma_f32_16x16x32_bf16 v[32:35], a[76:79], a[44:47], v[32:35]\n"
    "ds_read_b64_tr_b16 a[172:173], v23 offset:0\n"
    "ds_read_b64_tr_b16 a[174:175], v23 offset:4096\n"
    "v_mfma_f32_16x16x32_bf16 v[36:39], a[80:83], a[32:35], v[104:107]\n"
    "ds_read_b64_tr_b16 a[176:177], v24 offset:0\n"
    "ds_read_b64_tr_b16 a[178:179], v24 offset:4096\n"
    "v_mfma_f32_16x16x32_bf16 v[36:39], a[84:87], a[36:39], v[36:39]\n"
    "ds_read_b64_tr_b16 a[180:181], v25 offset:0\n"
    "ds_read_b64_tr_b16 a[182:183], v25 offset:4096\n"
    "s_waitcnt lgkmcnt(12)\n"
    "v_mfma_f32_16x16x32_bf16 v[36:39], a[88:91], a[40:43], v[36:39]\n"
    "ds_read_b64_tr_b16 a[184:185], v26 offset:0\n"
    "ds_read_b64_tr_b16 a[186:187], v26 offset:4096\n"
    "v_mfma_f32_16x16x32_bf16 v[36:39], a[92:95], a[44:47], v[36:39]\n"
    "ds_read_b64_tr_b16 a[188:189], v27 offset:0\n"
    "ds_read_b64_tr_b16 a[190:191], v27 offset:4096\n"
    "; -- t1\n"
    "v_mfma_f32_16x16x32_bf16 v[40:43], a[64:67], a[48:51], v[108:111]\n"
    "v_lshlrev_b32 v20, 16, v48\n"
    "v_and_b32 v21, 0xffff0000, v48\n"
    "v_mfma_f32_16x16x32_bf16 v[40:43], a[68:71], a[52:55], v[40:43]\n"
    "v_lshlrev_b32 v22, 16, v49\n"
    "v_and_b32 v23, 0xffff0000, v49\n"
    "v_mfma_f32_16x16x32_bf16 v[40:43], a[72:75], a[56:59], v[40:43]\n"
    "v_lshlrev_b32 v24, 16, v50\n"
    "v_and_b32 v25, 0xffff0000, v50\n"
    "v_mfma_f32_16x16x32_bf16 v[40:43], a[76:79], a[60:63], v[40:43]\n"
    "v_lshlrev_b32 v26, 16, v51\n"
    "v_and_b32 v27, 0xffff0000, v51\n"
    "v_mfma_f32_16x16x32_bf16 v[44:47], a[80:83], a[48:51], v[108:111]\n"
    "v_fma_f32 v20, v20, -s83, s83\n"
    "v_fma_f32 v21, v21, -s83, s83\n"
    "v_mfma_f32_16x16x32_bf16 v[44:47], a[84:87], a[52:55], v[44:47]\n"
    "v_fma_f32 v22, v22, -s83, s83\n"
    "v_fma_f32 v23, v23, -s83, s83\n"
    "v_mfma_f32_16x16x32_bf16 v[44:47], a[88:91], a[56:59], v[44:47]\n"
    "v_fma_f32 v24, v24, -s83, s83\n"
    "v_fma_f32 v25, v25, -s83, s83\n"
    "v_mfma_f32_16x16x32_bf16 v[44:47], a[92:95], a[60:63], v[44:47]\n"
    "v_fma_f32 v26, v26, -s83, s83\n"
    "v_fma_f32 v27, v27, -s83, s83\n"
    "v_add_f32 v32, v32, v20\n"
    "v_add_f32 v33, v33, v21\n"
    "v_add_f32 v34, v34, v22\n"
    "v_add_f32 v35, v35, v23\n"
    "v_add_f32 v36, v36, v24\n"
    "v_add_f32 v37, v37, v25\n"
    "v_add_f32 v38, v38, v26\n"
    "v_add_f32 v39, v39, v27\n"
    "v_lshlrev_b32 v20, 16, v52\n"
    "v_exp_f32 v32, v32\n"
    "v_and_b32 v21, 0xffff0000, v52\n"
    "v_lshlrev_b32 v22, 16, v53\n"
    "v_exp_f32 v33, v33\n"
    "v_and_b32 v23, 0xffff0000, v53\n"
    "v_lshlrev_b32 v24, 16, v54\n"
    "v_exp_f32 v34, v34\n"
    "v_and_b32 v25, 0xffff0000, v54\n"
    "v_lshlrev_b32 v26, 16, v55\n"
    "v_exp_f32 v35, v35\n"
    "v_and_b32 v27, 0xffff0000, v55\n"
    "v_fma_f32 v20, v20, -s83, s83\n"
    "v_exp_f32 v36, v36\n"
    "v_fma_f32 v21, v21, -s83, s83\n"
    "v_fma_f32 v22, v22, -s83, s83\n"
    "v_exp_f32 v37, v37\n"
    "v_fma_f32 v23, v23, -s83, s83\n"
    "v_fma_f32 v24, v24, -s83, s83\n"
    "v_exp_f32 v38, v38\n"
    "v_fma_f32 v25, v25, -s83, s83\n"
    "v_fma_f32 v26, v26, -s83, s83\n"
    "v_exp_f32 v39, v39\n"
    "v_fma_f32 v27, v27, -s83, s83\n"
    "v_cvt_pk_bf16_f32 v84, v32, v33\n"
    "v_cvt_pk_bf16_f32 v85, v34, v35\n"
    "v_cvt_pk_bf16_f32 v86, v36, v37\n"
    "v_cvt_pk_bf16_f32 v87, v38, v39\n"
    "; -- t2\n"
    "s_waitcnt lgkmcnt(0)\n"
    "s_nop 0\n"
    "v_mfma_f32_16x16x32_bf16 v[192:195], a[160:163], v[84:87], v[192:195]\n"
    "v_add_f32 v40, v40, v20\n"
    "v_add_f32 v41, v41, v21\n"
    "v_mfma_f32_16x16x32_bf16 v[196:199], a[164:167], v[84:87], v[196:199]\n"
    "v_add_f32 v42, v42, v22\n"
    "v_add_f32 v43, v43, v23\n"
    "v_mfma_f32_16x16x32_bf16 v[200:203], a[168:171], v[84:87], v[200:203]\n"
    "v_add_f32 v44, v44, v24\n"
    "v_add_f32 v45, v45, v25\n"
    "v_mfma_f32_16x16x32_bf16 v[204:207], a[172:175], v[84:87], v[204:207]\n"
    "v_add_f32 v46, v46, v26\n"
    "v_add_f32 v47, v47, v27\n"
    "v_mfma_f32_16x16x32_bf16 v[208:211], a[176:179], v[84:87], v[208:211]\n"
    "v_exp_f32 v40, v40\n"
    "v_exp_f32 v41, v41\n"
    "v_mfma_f32_16x16x32_bf16 v[212:215], a[180:183], v[84:87], v[212:215]\n"
    "v_exp_f32 v42, v42\n"
    "v_exp_f32 v43, v43\n"
    "v_mfma_f32_16x16x32_bf16 v[216:219], a[184:187], v[84:87], v[216:219]\n"
    "v_exp_f32 v44, v44\n"
    "v_exp_f32 v45, v45\n"
    "v_mfma_f32_16x16x32_bf16 v[220:223], a[188:191], v[84:87], v[220:223]\n"
    "v_exp_f32 v46, v46\n"
    "v_exp_f32 v47, v47\n"
    "v_mov_b32 v20, 0x3f003f00\n"
    "v_mov_b32 v21, 0x3f003f00\n"
    "v_mov_b32 v22, 0x3f003f00\n"
    "v_mov_b32 v23, 0x3f003f00\n"
    "v_cvt_pk_bf16_f32 v92, v40, v41\n"
    "v_cvt_pk_bf16_f32 v93, v42, v43\n"
    "v_cvt_pk_bf16_f32 v94, v44, v45\n"
    "v_cvt_pk_bf16_f32 v95, v46, v47\n"
    "v_mfma_f32_16x16x32_bf16 a[200:203], v[20:23], v[84:87], a[200:203]\n"
    "s_nop 0\n"
    "v_mfma_f32_16x16x32_bf16 v[224:227], a[160:163], v[92:95], v[224:227]\n"
    "v_mfma_f32_16x16x32_bf16 v[228:231], a[164:167], v[92:95], v[228:231]\n"
    "v_mfma_f32_16x16x32_bf16 v[232:235], a[168:171], v[92:95], v[232:235]\n"
    "v_mfma_f32_16x16x32_bf16 v[236:239], a[172:175], v[92:95], v[236:239]\n"
    "v_mfma_f32_16x16x32_bf16 v[240:243], a[176:179], v[92:95], v[240:243]\n"
    "v_mfma_f32_16x16x32_bf16 v[244:247], a[180:183], v[92:95], v[244:247]\n"
    "v_mfma_f32_16x16x32_bf16 v[248:251], a[184:187], v[92:95], v[248:251]\n"
    "v_mfma_f32_16x16x32_bf16 v[252:255], a[188:191], v[92:95], v[252:255]\n"
    "v_mfma_f32_16x16x32_bf16 a[204:207], v[20:23], v[92:95], a[204:207]\n"
    "Ltskip%=:\n"
    "Lh3_%=:\n"
    "s_cbranch_execz Lr3_%=\n"
    "; -- h30a\n"
    "v_mfma_f32_16x16x32_bf16 v[32:35], a[96:99], a[0:3], v[96:99]\n"
    "v_exp_f32 v56, v56\n"
    "v_mfma_f32_16x16x32_bf16 v[32:35], a[100:103], a[4:7], v[32:35]\n"
    "v_exp_f32 v57, v57\n"
    "v_mfma_f32_16x16x32_bf16 v[32:35], a[104:107], a[8:11], v[32:35]\n"
    "v_exp_f32 v58, v58\n"
    "v_mfma_f32_16x16x32_bf16 v[32:35], a[108:111], a[12:15], v[32:35]\n"
    "v_mfma_f32_16x16x32_bf16 v[36:39], a[112:115], a[0:3], v[96:99]\n"
    "s_mov_b32 s80, 589824\n"
    "s_add_u32 m0, %[m0w], 8192\n"
    "s_nop 0\n"
    "buffer_load_dwordx4 %[voffk], %[srdk], s80 offen nt lds\n"
    "v_cvt_pk_bf16_f32 v88, v56, v57\n"
    "v_mfma_f32_16x16x32_bf16 v[36:39], a[116:119], a[4:7], v[36:39]\n"
    "v_exp_f32 v59, v59\n"
    "v_mfma_f32_16x16x32_bf16 v[36:39], a[120:123], a[8:11], v[36:39]\n"
    "v_exp_f32 v60, v60\n"
    "v_mfma_f32_16x16x32_bf16 v[36:39], a[124:127], a[12:15], v[36:39]\n"
    "; -- h30b\n"
    "v_mfma_f32_16x16x32_bf16 v[128:131], a[128:131], v[64:67], v[128:131]\n"
    "v_cvt_pk_bf16_f32 v89, v58, v59\n"
    "v_mfma_f32_16x16x32_bf16 v[132:135], a[132:135], v[64:67], v[132:135]\n"
    "v_exp_f32 v61, v61\n"
    "v_mfma_f32_16x16x32_bf16 v[136:139], a[136:139], v[64:67], v[136:139]\n"
    "v_exp_f32 v62, v62\n"
    "v_mfma_f32_16x16x32_bf16 v[140:143], a[140:143], v[64:67], v[140:143]\n"
    "v_mfma_f32_16x16x32_bf16 v[144:147], a[144:147], v[64:67], v[144:147]\n"
    "v_cvt_pk_bf16_f32 v90, v60, v61\n"
    "v_mfma_f32_16x16x32_bf16 v[148:151], a[148:151], v[64:67], v[148:151]\n"
    "v_exp_f32 v63, v63\n"
    "v_mfma_f32_16x16x32_bf16 v[152:155], a[152:155], v[64:67], v[152:155]\n"
    "v_cvt_pk_bf16_f32 v91, v62, v63\n"
    "v_mfma_f32_16x16x32_bf16 v[156:159], a[156:159], v[64:67], v[156:159]\n"
    "v_mfma_f32_16x16x32_bf16 v[40:43], a[96:99], a[16:19], v[100:103]\n"
    "s_waitcnt vmcnt(25)\n"
    "s_barrier\n"
    "; -- h31a\n"
    "v_mfma_f32_16x16x32_bf16 v[40:43], a[100:103], a[20:23], v[40:43]\n"
    "ds_read_b128 a[64:67], v112 offset:32768\n"
    "v_exp_f32 v32, v32\n"
    "v_mfma_f32_16x16x32_bf16 v[40:43], a[104:107], a[24:27], v[40:43]\n"
    "ds_read_b128 a[68:71], v113 offset:32768\n"
    "v_exp_f32 v33, v33\n"
    "v_mfma_f32_16x16x32_bf16 v[40:43], a[108:111], a[28:31], v[40:43]\n"
    "ds_read_b128 a[72:75], v114 offset:32768\n"
    "v_exp_f32 v34, v34\n"
    "v_mfma_f32_16x16x32_bf16 v[44:47], a[112:115], a[16:19], v[100:103]\n"
    "buffer_load_dwordx4 v124, %[srdk], s80 offen offset:1024 nt lds\n"
    "v_cvt_pk_bf16_f32 v68, v32, v33\n"
    "v_mfma_f32_16x16x32_bf16 v[44:47], a[116:119], a[20:23], v[44:47]\n"
    "ds_read_b128 a[76:79], v115 offset:32768\n"
    "v_exp_f32 v35, v35\n"
    "v_mfma_f32_16x16x32_bf16 v[44:47], a[120:123], a[24:27], v[44:47]\n"
    "ds_read_b128 a[80:83], v112 offset:36864\n"
    "v_exp_f32 v36, v36\n"
    "v_mfma_f32_16x16x32_bf16 v[44:47], a[124:127], a[28:31], v[44:47]\n"
    "ds_read_b128 a[84:87], v113 offset:36864\n"
    "; -- h31b\n"
    "v_mfma_f32_16x16x32_bf16 v[160:163], a[128:131], v[72:75], v[160:163]\n"
    "ds_read_b128 a[88:91], v114 offset:36864\n"
    "v_cvt_pk_bf16_f32 v69, v34, v35\n"
    "v_mfma_f32_16x16x32_bf16 v[164:167], a[132:135], v[72:75], v[164:167]\n"
    "v_exp_f32 v37, v37\n"
    "v_mfma_f32_16x16x32_bf16 v[168:171], a[136:139], v[72:75], v[168:171]\n"
    "ds_read_b128 a[92:95], v115 offset:36864\n"
    "v_exp_f32 v38, v38\n"
    "v_mfma_f32_16x16x32_bf16 v[172:175], a[140:143], v[72:75], v[172:175]\n"
    "ds_read_b64_tr_b16 a[160:161], v116 offset:24576\n"
    "v_mfma_f32_16x16x32_bf16 v[176:179], a[144:147], v[72:75], v[176:179]\n"
    "ds_read_b64_tr_b16 a[162:163], v116 offset:28672\n"
    "v_cvt_pk_bf16_f32 v70, v36, v37\n"
    "v_mfma_f32_16x16x32_bf16 v[180:183], a[148:151], v[72:75], v[180:183]\n"
    "v_exp_f32 v39, v39\n"
    "v_mfma_f32_16x16x32_bf16 v[184:187], a[152:155], v[72:75], v[184:187]\n"
    "ds_read_b64_tr_b16 a[164:165], v117 offset:24576\n"
    "v_cvt_pk_bf16_f32 v71, v38, v39\n"
    "v_mfma_f32_16x16x32_bf16 v[188:191], a[156:159], v[72:75], v[188:191]\n"
    "ds_read_b64_tr_b16 a[166:167], v117 offset:28672\n"
    "; -- h32a\n"
    "v_mfma_f32_16x16x32_bf16 v[48:51], a[96:99], a[32:35], v[104:107]\n"
    "ds_read_b64_tr_b16 a[168:169], v118 offset:24576\n"
    "v_exp_f32 v40, v40\n"
    "v_mfma_f32_16x16x32_bf16 v[48:51], a[100:103], a[36:39], v[48:51]\n"
    "v_exp_f32 v41, v41\n"
    "v_mfma_f32_16x16x32_bf16 v[48:51], a[104:107], a[40:43], v[48:51]\n"
    "ds_read_b64_tr_b16 a[170:171], v118 offset:28672\n"
    "v_exp_f32 v42, v42\n"
    "v_mfma_f32_16x16x32_bf16 v[48:51], a[108:111], a[44:47], v[48:51]\n"
    "ds_read_b64_tr_b16 a[172:173], v119 offset:24576\n"
    "v_mfma_f32_16x16x32_bf16 v[52:55], a[112:115], a[32:35], v[104:107]\n"
    "s_add_u32 m0, %[m0w], 65536\n"
    "s_nop 0\n"
    "buffer_load_dwordx4 %[voffv], %[srdv], s80 offen nt lds\n"
    "ds_read_b64_tr_b16 a[174:175], v119 offset:28672\n"
    "v_cvt_pk_bf16_f32 v76, v40, v41\n"
    "v_mfma_f32_16x16x32_bf16 v[52:55], a[116:119], a[36:39], v[52:55]\n"
    "v_exp_f32 v43, v43\n"
    "v_mfma_f32_16x16x32_bf16 v[52:55], a[120:123], a[40:43], v[52:55]\n"
    "ds_read_b64_tr_b16 a[176:177], v120 offset:24576\n"
    "v_exp_f32 v44, v44\n"
    "v_mfma_f32_16x16x32_bf16 v[52:55], a[124:127], a[44:47], v[52:55]\n"
    "ds_read_b64_tr_b16 a[178:179], v120 offset:28672\n"
    "; -- h32b\n"
    "v_mfma_f32_16x16x32_bf16 v[192:195], a[128:131], v[80:83], v[192:195]\n"
    "ds_read_b64_tr_b16 a[180:181], v121 offset:24576\n"
    "v_cvt_pk_bf16_f32 v77, v42, v43\n"
    "v_mfma_f32_16x16x32_bf16 v[196:199], a[132:135], v[80:83], v[196:199]\n"
    "v_exp_f32 v45, v45\n"
    "v_mfma_f32_16x16x32_bf16 v[200:203], a[136:139], v[80:83], v[200:203]\n"
    "ds_read_b64_tr_b16 a[182:183], v121 offset:28672\n"
    "v_exp_f32 v46, v46\n"
    "v_mfma_f32_16x16x32_bf16 v[204:207], a[140:143], v[80:83], v[204:207]\n"
    "ds_read_b64_tr_b16 a[184:185], v122 offset:24576\n"
    "v_mfma_f32_16x16x32_bf16 v[208:211], a[144:147], v[80:83], v[208:211]\n"
    "ds_read_b64_tr_b16 a[186:187], v122 offset:28672\n"
    "v_cvt_pk_bf16_f32 v78, v44, v45\n"
    "v_mfma_f32_16x16x32_bf16 v[212:215], a[148:151], v[80:83], v[212:215]\n"
    "v_exp_f32 v47, v47\n"
    "v_mfma_f32_16x16x32_bf16 v[216:219], a[152:155], v[80:83], v[216:219]\n"
    "ds_read_b64_tr_b16 a[188:189], v123 offset:24576\n"
    "v_cvt_pk_bf16_f32 v79, v46, v47\n"
    "v_mfma_f32_16x16x32_bf16 v[220:223], a[156:159], v[80:83], v[220:223]\n"
    "ds_read_b64_tr_b16 a[190:191], v123 offset:28672\n"
    "; -- h33a\n"
    "v_mfma_f32_16x16x32_bf16 v[56:59], a[96:99], a[48:51], v[108:111]\n"
    "v_exp_f32 v48, v48\n"
    "v_mfma_f32_16x16x32_bf16 v[56:59], a[100:103], a[52:55], v[56:59]\n"
    "v_exp_f32 v49, v49\n"
    "v_mfma_f32_16x16x32_bf16 v[56:59], a[104:107], a[56:59], v[56:59]\n"
    "v_exp_f32 v50, v50\n"
    "v_mfma_f32_16x16x32_bf16 v[56:59], a[108:111], a[60:63], v[56:59]\n"
    "v_mfma_f32_16x16x32_bf16 v[60:63], a[112:115], a[48:51], v[108:111]\n"
    "buffer_load_dwordx4 v125, %[srdv], s80 offen offset:1024 nt lds\n"
    "v_cvt_pk_bf16_f32 v84, v48, v49\n"
    "v_mfma_f32_16x16x32_bf16 v[60:63], a[116:119], a[52:55], v[60:63]\n"
    "v_exp_f32 v51, v51\n"
    "v_mfma_f32_16x16x32_bf16 v[60:63], a[120:123], a[56:59], v[60:63]\n"
    "v_exp_f32 v52, v52\n"
    "v_mfma_f32_16x16x32_bf16 v[60:63], a[124:127], a[60:63], v[60:63]\n"
    "; -- h33b\n"
    "v_mfma_f32_16x16x32_bf16 v[224:227], a[128:131], v[88:91], v[224:227]\n"
    "v_cvt_pk_bf16_f32 v85, v50, v51\n"
    "v_mfma_f32_16x16x32_bf16 v[228:231], a[132:135], v[88:91], v[228:231]\n"
    "v_exp_f32 v53, v53\n"
    "v_mfma_f32_16x16x32_bf16 v[232:235], a[136:139], v[88:91], v[232:235]\n"
    "v_exp_f32 v54, v54\n"
    "v_mfma_f32_16x16x32_bf16 v[236:239], a[140:143], v[88:91], v[236:239]\n"
    "v_mfma_f32_16x16x32_bf16 v[240:243], a[144:147], v[88:91], v[240:243]\n"
    "v_cvt_pk_bf16_f32 v86, v52, v53\n"
    "v_mfma_f32_16x16x32_bf16 v[244:247], a[148:151], v[88:91], v[244:247]\n"
    "v_exp_f32 v55, v55\n"
    "v_mfma_f32_16x16x32_bf16 v[248:251], a[152:155], v[88:91], v[248:251]\n"
    "v_cvt_pk_bf16_f32 v87, v54, v55\n"
    "v_mfma_f32_16x16x32_bf16 v[252:255], a[156:159], v[88:91], v[252:255]\n"
    "; ---- head unit 4\n"
    "s_waitcnt lgkmcnt(0)\n"
    "; -- h40a\n"
    "v_mfma_f32_16x16x32_bf16 v[32:35], a[64:67], a[0:3], v[96:99]\n"
    "s_mov_b32 s86, 0xffff0000\n"
    "v_exp_f32 v56, v56\n"
    "s_mov_b32 s87, 0xffff0000\n"
    "v_mfma_f32_16x16x32_bf16 v[32:35], a[68:71], a[4:7], v[32:35]\n"
    "v_mov_b32 v20, 0x4e800000\n"
    "v_lshrrev_b32 v24, 1, v127\n"
    "v_exp_f32 v57, v57\n"
    "v_mfma_f32_16x16x32_bf16 v[32:35], a[72:75], a[8:11], v[32:35]\n"
    "v_and_b32 v24, 0x78, v24\n"
    "v_add_u32 v21, 68608, v24\n"
    "v_cndmask_b32 v21, v24, v21, s[86:87]\n"
    "v_mfma_f32_16x16x32_bf16 v[32:35], a[76:79], a[12:15], v[32:35]\n"
    "v_exp_f32 v58, v58\n"
    "v_sub_u32 v18, v21, v24\n"
    "v_mfma_f32_16x16x32_bf16 v[36:39], a[80:83], a[0:3], v[96:99]\n"
    "s_mov_b32 s80, 655360\n"
    "s_add_u32 m0, %[m0w], 16384\n"
    "s_nop 0\n"
    "buffer_load_dwordx4 %[voffk], %[srdk], s80 offen nt lds\n"
    "v_lshlrev_b32 v25, 5, v24\n"
    "v_cvt_pk_bf16_f32 v92, v56, v57\n"
    "v_and_b32 v22, 0x38, v24\n"
    "v_mfma_f32_16x16x32_bf16 v[36:39], a[84:87], a[4:7], v[36:39]\n"
    "v_add_u32 v24, 8192, v24\n"
    "v_mul_u32_u24 v22, 1072, v22\n"
    "v_exp_f32 v59, v59\n"
    "v_mfma_f32_16x16x32_bf16 v[36:39], a[88:91], a[8:11], v[36:39]\n"
    "v_add_u32 v22, 8448, v22\n"
    "s_lshr_b32 s83, %[m0w], 1\n"
    "v_add_u32 v23, s83, v127\n"
    "v_mfma_f32_16x16x32_bf16 v[36:39], a[92:95], a[12:15], v[36:39]\n"
    "v_exp_f32 v60, v60\n"
    "v_lshrrev_b32 v19, 1, v127\n"
    "; -- h40b\n"
    "v_mfma_f32_16x16x32_bf16 v[128:131], a[160:163], v[68:71], v[128:131]\n"
    "s_add_u32 s83, s83, 155648\n"
    "v_cvt_pk_bf16_f32 v93, v58, v59\n"
    "v_add_u32 v19, s83, v19\n"
    "v_mfma_f32_16x16x32_bf16 v[132:135], a[164:167], v[68:71], v[132:135]\n"
    "v_lshrrev_b32 v28, 5, v127\n"
    "v_and_b32 v28, 0x18, v28\n"
    "v_mfma_f32_16x16x32_bf16 v[136:139], a[168:171], v[68:71], v[136:139]\n"
    "v_exp_f32 v61, v61\n"
    "s_lshr_b32 s83, %[m0w], 5\n"
    "v_add3_u32 v25, v25, v28, s83\n"
    "v_mfma_f32_16x16x32_bf16 v[140:143], a[172:175], v[68:71], v[140:143]\n"
    "s_mov_b32 %[bad], 0\n"
    "v_exp_f32 v62, v62\n"
    "v_mfma_f32_16x16x32_bf16 v[144:147], a[176:179], v[68:71], v[144:147]\n"
    "s_cmp_lg_u32 %[mode], 0\ns_cselect_b32 s78, 0, 3\n"
    "s_lshl_b32 s88, 1, %[sp]\n"
    "v_cvt_pk_bf16_f32 v94, v60, v61\n"
    "v_mfma_f32_16x16x32_bf16 v[148:151], a[180:183], v[68:71], v[148:151]\n"
    "s_or_b32 s88, s88, 0xffffff00\n"
    "s_mov_b32 s89, -1\n"
    "v_mfma_f32_16x16x32_bf16 v[152:155], a[184:187], v[68:71], v[152:155]\n"
    "s_lshr_b32 s90, %[sp], 1\n"
    "v_exp_f32 v63, v63\n"
    "s_lshl_b32 s90, s90, 10\n"
    "v_mfma_f32_16x16x32_bf16 v[156:159], a[188:191], v[68:71], v[156:159]\n"
    "s_and_b32 s91, %[sp], 1\n"
    "s_lshl_b32 s91, s91, 7\n"
    "v_smfmac_f32_16x16x64_bf16 a[192:195], a[208:211], v[64:71], v126\n"
    "v_cvt_pk_bf16_f32 v95, v62, v63\n"
    "s_add_u32 s90, s90, s91\n"
    "s_nop 1\n"
    "v_mfma_f32_16x16x32_bf16 v[40:43], a[64:67], a[16:19], v[100:103]\n"
    "s_waitcnt vmcnt(9)\n"
    "s_barrier\n"
    "; -- h41a\n"
    "v_mfma_f32_16x16x32_bf16 v[40:43], a[68:71], a[20:23], v[40:43]\n"
    "ds_read_b128 a[96:99], v112 offset:40960\n"
    "v_exp_f32 v32, v32\n"
    "v_mfma_f32_16x16x32_bf16 v[40:43], a[72:75], a[24:27], v[40:43]\n"
    "ds_read_b128 a[100:103], v113 offset:40960\n"
    "v_exp_f32 v33, v33\n"
    "v_mfma_f32_16x16x32_bf16 v[40:43], a[76:79], a[28:31], v[40:43]\n"
    "ds_read_b128 a[104:107], v114 offset:40960\n"
    "v_exp_f32 v34, v34\n"
    "v_mfma_f32_16x16x32_bf16 v[44:47], a[80:83], a[16:19], v[100:103]\n"
    "buffer_load_dwordx4 v124, %[srdk], s80 offen offset:1024 nt lds\n"
    "v_cvt_pk_bf16_f32 v64, v32, v33\n"
    "v_mfma_f32_16x16x32_bf16 v[44:47], a[84:87], a[20:23], v[44:47]\n"
    "ds_read_b128 a[108:111], v115 offset:40960\n"
    "v_exp_f32 v35, v35\n"
    "v_mfma_f32_16x16x32_bf16 v[44:47], a[88:91], a[24:27], v[44:47]\n"
    "Lh4_%=:\n"
    "s_cbranch_execz Lr4_%=\n"
    "ds_read_b128 a[112:115], v112 offset:45056\n"
    "v_exp_f32 v36, v36\n"
    "v_mfma_f32_16x16x32_bf16 v[44:47], a[92:95], a[28:31], v[44:47]\n"
    "ds_read_b128 a[116:119], v113 offset:45056\n"
    "; -- h41b\n"
    "v_mfma_f32_16x16x32_bf16 v[160:163], a[160:163], v[76:79], v[160:163]\n"
    "ds_read_b128 a[120:123], v114 offset:45056\n"
    "v_cvt_pk_bf16_f32 v65, v34, v35\n"
    "v_mfma_f32_16x16x32_bf16 v[164:167], a[164:167], v[76:79], v[164:167]\n"
    "v_exp_f32 v37, v37\n"
    "v_mfma_f32_16x16x32_bf16 v[168:171], a[168:171], v[76:79], v[168:171]\n"
    "ds_read_b128 a[124:127], v115 offset:45056\n"
    "v_mfma_f32_16x16x32_bf16 v[172:175], a[172:175], v[76:79], v[172:175]\n"
    "ds_read_b64_tr_b16 a[128:129], v116 offset:32768\n"
    "v_exp_f32 v38, v38\n"
    "v_mfma_f32_16x16x32_bf16 v[176:179], a[176:179], v[76:79], v[176:179]\n"
    "v_cvt_pk_bf16_f32 v66, v36, v37\n"
    "v_mfma_f32_16x16x32_bf16 v[180:183], a[180:183], v[76:79], v[180:183]\n"
    "ds_read_b64_tr_b16 a[130:131], v116 offset:36864\n"
    "v_mfma_f32_16x16x32_bf16 v[184:187], a[184:187], v[76:79], v[184:187]\n"
    "ds_read_b64_tr_b16 a[132:133], v117 offset:32768\n"
    "v_exp_f32 v39, v39\n"
    "v_mfma_f32_16x16x32_bf16 v[188:191], a[188:191], v[76:79], v[188:191]\n"
    "v_cvt_pk_bf16_f32 v67, v38, v39\n"
    "v_smfmac_f32_16x16x64_bf16 a[196:199], a[208:211], v[72:79], v126\n"
    "ds_read_b64_tr_b16 a[134:135], v117 offset:36864\n"
    "s_nop 1\n"
    "; -- h42a\n"
    "v_mfma_f32_16x16x32_bf16 v[48:51], a[64:67], a[32:35], v[104:107]\n"
    "ds_read_b64_tr_b16 a[136:137], v118 offset:32768\n"
    "s_add_u32 s90, s90, 147456\n"
    "v_exp_f32 v40, v40\n"
    "s_lshr_b32 s91, %[m0w], 9\n"
    "v_mfma_f32_16x16x32_bf16 v[48:51], a[68:71], a[36:39], v[48:51]\n"
    "s_add_u32 s91, s91, 163776\n"
    "s_add_u32 s56, %[sp], 1\n"
    "v_exp_f32 v41, v41\n"
    "v_mfma_f32_16x16x32_bf16 v[48:51], a[72:75], a[40:43], v[48:51]\n"
    "ds_read_b64_tr_b16 a[138:139], v118 offset:36864\n"
    "s_and_b32 s56, s56, 7\n"
    "s_mul_i32 s56, s56, 8576\n"
    "v_exp_f32 v42, v42\n"
    "v_mfma_f32_16x16x32_bf16 v[48:51], a[76:79], a[44:47], v[48:51]\n"
    "ds_read_b64_tr_b16 a[140:141], v119 offset:32768\n"
    "s_add_u32 s56, s56, %[cbase]\n"
    "s_add_u32 s57, %[sp], 2\n"
    "v_mfma_f32_16x16x32_bf16 v[52:55], a[80:83], a[32:35], v[104:107]\n"
    "s_add_u32 m0, %[m0w], 73728\n"
    "s_nop 0\n"
    "buffer_load_dwordx4 %[voffv], %[srdv], s80 offen nt lds\n"
    "ds_read_b64_tr_b16 a[142:143], v119 offset:36864\n"
    "s_and_b32 s57, s57, 7\n"
    "v_cvt_pk_bf16_f32 v72, v40, v41\n"
    "v_mfma_f32_16x16x32_bf16 v[52:55], a[84:87], a[36:39], v[52:55]\n"
    "s_mul_i32 s57, s57, 8576\n"
    "s_add_u32 s57, s57, %[cbase]\n"
    "s_add_u32 s58, %[sp], 3\n"
    "v_exp_f32 v43, v43\n"
    "v_mfma_f32_16x16x32_bf16 v[52:55], a[88:91], a[40:43], v[52:55]\n"
    "ds_read_b64_tr_b16 a[144:145], v120 offset:32768\n"
    "s_and_b32 s58, s58, 7\n"
    "s_mul_i32 s58, s58, 8576\n"
    "v_mfma_f32_16x16x32_bf16 v[52:55], a[92:95], a[44:47], v[52:55]\n"
    "ds_read_b64_tr_b16 a[146:147], v120 offset:36864\n"
    "v_exp_f32 v44, v44\n"
    "s_add_u32 s58, s58, %[cbase]\n"
    "; -- h42b\n"
    "v_mfma_f32_16x16x32_bf16 v[192:195], a[160:163], v[84:87], v[192:195]\n"
    "ds_read_b64_tr_b16 a[148:149], v121 offset:32768\n"
    "s_add_u32 s59, %[sp], 4\n"
    "s_and_b32 s59, s59, 7\n"
    "v_cvt_pk_bf16_f32 v73, v42, v43\n"
    "v_mfma_f32_16x16x32_bf16 v[196:199], a[164:167], v[84:87], v[196:199]\n"
    "s_mul_i32 s59, s59, 8576\n"
    "s_add_u32 s59, s59, %[cbase]\n"
    "v_exp_f32 v45, v45\n"
    "v_mfma_f32_16x16x32_bf16 v[200:203], a[168:171], v[84:87], v[200:203]\n"
    "ds_read_b64_tr_b16 a[150:151], v121 offset:36864\n"
    "s_add_u32 s60, %[sp], 5\n"
    "s_and_b32 s60, s60, 7\n"
    "v_mfma_f32_16x16x32_bf16 v[204:207], a[172:175], v[84:87], v[204:207]\n"
    "ds_read_b64_tr_b16 a[152:153], v122 offset:32768\n"
    "s_mul_i32 s60, s60, 8576\n"
    "v_exp_f32 v46, v46\n"
    "v_mfma_f32_16x16x32_bf16 v[208:211], a[176:179], v[84:87], v[208:211]\n"
    "s_add_u32 s60, s60, %[cbase]\n"
    "s_add_u32 s61, %[sp], 6\n"
    "s_and_b32 s61, s61, 7\n"
    "v_mfma_f32_16x16x32_bf16 v[212:215], a[180:183], v[84:87], v[212:215]\n"
    "ds_read_b64_tr_b16 a[154:155], v122 offset:36864\n"
    "v_cvt_pk_bf16_f32 v74, v44, v45\n"
    "s_mul_i32 s61, s61, 8576\n"
    "v_mfma_f32_16x16x32_bf16 v[216:219], a[184:187], v[84:87], v[216:219]\n"
    "ds_read_b64_tr_b16 a[156:157], v123 offset:32768\n"
    "s_add_u32 s61, s61, %[cbase]\n"
    "v_exp_f32 v47, v47\n"
    "v_mfma_f32_16x16x32_bf16 v[220:223], a[188:191], v[84:87], v[220:223]\n"
    "s_add_u32 s62, %[sp], 7\n"
    "s_and_b32 s62, s62, 7\n"
    "s_mul_i32 s62, s62, 8576\n"
    "v_smfmac_f32_16x16x64_bf16 a[200:203], a[208:211], v[80:87], v126\n"
    "ds_read_b64_tr_b16 a[158:159], v123 offset:36864\n"
    "v_cvt_pk_bf16_f32 v75, v46, v47\n"
    "s_add_u32 s62, s62, %[cbase]\n"
    "s_nop 1\n"
    "; -- h43a\n"
    "v_mfma_f32_16x16x32_bf16 v[56:59], a[64:67], a[48:51], v[108:111]\n"
    "v_exp_f32 v48, v48\n"
    "v_accvgpr_read_b32 v26, a212\n"
    "v_mfma_f32_16x16x32_bf16 v[56:59], a[68:71], a[52:55], v[56:59]\n"
    "v_exp_f32 v49, v49\n"
    "v_mfma_f32_16x16x32_bf16 v[56:59], a[72:75], a[56:59], v[56:59]\n"
    "v_accvgpr_read_b32 v27, a213\n"
    "v_exp_f32 v50, v50\n"
    "v_mfma_f32_16x16x32_bf16 v[56:59], a[76:79], a[60:63], v[56:59]\n"
    "v_mov_b32 v28, 0x9e3779b9\n"
    "v_mfma_f32_16x16x32_bf16 v[60:63], a[80:83], a[48:51], v[108:111]\n"
    "buffer_load_dwordx4 v125, %[srdv], s80 offen offset:1024 nt lds\n"
    "v_cvt_pk_bf16_f32 v80, v48, v49\n"
    "v_mfma_f32_16x16x32_bf16 v[60:63], a[84:87], a[52:55], v[60:63]\n"
    "v_add_co_u32 v26, vcc, 0x7f4a7c15, v26\n"
    "v_exp_f32 v51, v51\n"
    "v_mfma_f32_16x16x32_bf16 v[60:63], a[88:91], a[56:59], v[60:63]\n"
    "s_nop 1\n"
    "v_mfma_f32_16x16x32_bf16 v[60:63], a[92:95], a[60:63], v[60:63]\n"
    "v_exp_f32 v52, v52\n"
    "; -- h43b\n"
    "v_mfma_f32_16x16x32_bf16 v[224:227], a[160:163], v[92:95], v[224:227]\n"
    "v_cvt_pk_bf16_f32 v81, v50, v51\n"
    "v_addc_co_u32 v27, vcc, v28, v27, vcc\n"
    "v_mfma_f32_16x16x32_bf16 v[228:231], a[164:167], v[92:95], v[228:231]\n"
    "v_exp_f32 v53, v53\n"
    "v_mfma_f32_16x16x32_bf16 v[232:235], a[168:171], v[92:95], v[232:235]\n"
    "v_cndmask_b32 %[taglo], v26, %[taglo], s[94:95]\n"
    "v_mfma_f32_16x16x32_bf16 v[236:239], a[172:175], v[92:95], v[236:239]\n"
    "v_exp_f32 v54, v54\n"
    "v_mfma_f32_16x16x32_bf16 v[240:243], a[176:179], v[92:95], v[240:243]\n"
    "v_cndmask_b32 %[taghi], v27, %[taghi], s[94:95]\n"
    "v_cvt_pk_bf16_f32 v82, v52, v53\n"
    "v_mfma_f32_16x16x32_bf16 v[244:247], a[180:183], v[92:95], v[244:247]\n"
    "v_mov_b32 v26, %[taglo]\n"
    "v_mfma_f32_16x16x32_bf16 v[248:251], a[184:187], v[92:95], v[248:251]\n"
    "v_exp_f32 v55, v55\n"
    "v_mfma_f32_16x16x32_bf16 v[252:255], a[188:191], v[92:95], v[252:255]\n"
    "v_mov_b32 v27, %[taghi]\n"
    "v_smfmac_f32_16x16x64_bf16 a[204:207], a[208:211], v[88:95], v126\n"
    "v_cvt_pk_bf16_f32 v83, v54, v55\n"
    "s_nop 1\n"
    "s_waitcnt vmcnt(19)\n"
    "s_barrier\n"
    "s_mov_b32 s80, 720896\n"
    "Lloop%=:\n"
    "; ---- unit 5\n"
    "s_waitcnt lgkmcnt(0)\n"
    "v_mfma_f32_16x16x32_bf16 v[32:35], a[96:99], a[0:3], v[96:99]\n"
    "v_exp_f32 v56, v56\n"
    "v_mfma_f32_16x16x32_bf16 v[32:35], a[100:103], a[4:7], v[32:35]\n"
    "s_add_u32 m0, %[m0w], 24576\n"
    "ds_read_b128 a[64:67], v112 offset:49152\n"
    "v_mfma_f32_16x16x32_bf16 v[32:35], a[104:107], a[8:11], v[32:35]\n"
    "v_exp_f32 v57, v57\n"
    "v_mfma_f32_16x16x32_bf16 v[32:35], a[108:111], a[12:15], v[32:35]\n"
    "ds_read_b128 a[68:71], v113 offset:49152\n"
    "v_cvt_pk_bf16_f32 v88, v56, v57\n"
    "v_mfma_f32_16x16x32_bf16 v[36:39], a[112:115], a[0:3], v[96:99]\n"
    "v_exp_f32 v58, v58\n"
    "v_mfma_f32_16x16x32_bf16 v[36:39], a[116:119], a[4:7], v[36:39]\n"
    "buffer_load_dwordx4 %[voffk], %[srdk], s80 offen nt lds\n"
    "v_mfma_f32_16x16x32_bf16 v[36:39], a[120:123], a[8:11], v[36:39]\n"
    "v_exp_f32 v59, v59\n"
    "v_mfma_f32_16x16x32_bf16 v[36:39], a[124:127], a[12:15], v[36:39]\n"
    "ds_read_b128 a[72:75], v114 offset:49152\n"
    "v_cvt_pk_bf16_f32 v89, v58, v59\n"
    "v_mfma_f32_16x16x32_bf16 v[128:131], a[128:131], v[64:67], v[128:131]\n"
    "v_exp_f32 v60, v60\n"
    "v_mfma_f32_16x16x32_bf16 v[132:135], a[132:135], v[64:67], v[132:135]\n"
    "ds_read_b128 a[76:79], v115 offset:49152\n"
    "v_mfma_f32_16x16x32_bf16 v[136:139], a[136:139], v[64:67], v[136:139]\n"
    "v_exp_f32 v61, v61\n"
    "v_mfma_f32_16x16x32_bf16 v[140:143], a[140:143], v[64:67], v[140:143]\n"
    "ds_read_b128 a[80:83], v112 offset:53248\n"
    "v_cvt_pk_bf16_f32 v90, v60, v61\n"
    "v_mfma_f32_16x16x32_bf16 v[144:147], a[144:147], v[64:67], v[144:147]\n"
    "v_exp_f32 v62, v62\n"
    "v_mfma_f32_16x16x32_bf16 v[148:151], a[148:151], v[64:67], v[148:151]\n"
    "ds_read_b128 a[84:87], v113 offset:53248\n"
    "v_mfma_f32_16x16x32_bf16 v[152:155], a[152:155], v[64:67], v[152:155]\n"
    "v_exp_f32 v63, v63\n"
    "v_mfma_f32_16x16x32_bf16 v[156:159], a[156:159], v[64:67], v[156:159]\n"
    "ds_read_b128 a[88:91], v114 offset:53248\n"
    "v_cvt_pk_bf16_f32 v91, v62, v63\n"
    "v_mfma_f32_16x16x32_bf16 v[40:43], a[96:99], a[16:19], v[100:103]\n"
    "v_exp_f32 v32, v32\n"
    "v_mfma_f32_16x16x32_bf16 v[40:43], a[100:103], a[20:23], v[40:43]\n"
    "ds_read_b128 a[92:95], v115 offset:53248\n"
    "v_mfma_f32_16x16x32_bf16 v[40:43], a[104:107], a[24:27], v[40:43]\n"
    "v_exp_f32 v33, v33\n"
    "v_mfma_f32_16x16x32_bf16 v[40:43], a[108:111], a[28:31], v[40:43]\n"
    "ds_read_b64_tr_b16 a[160:161], v116 offset:40960\n"
    "v_cvt_pk_bf16_f32 v68, v32, v33\n"
    "v_mfma_f32_16x16x32_bf16 v[44:47], a[112:115], a[16:19], v[100:103]\n"
    "v_exp_f32 v34, v34\n"
    "v_mfma_f32_16x16x32_bf16 v[44:47], a[116:119], a[20:23], v[44:47]\n"
    "buffer_load_dwordx4 v124, %[srdk], s80 offen offset:1024 nt lds\n"
    "v_mfma_f32_16x16x32_bf16 v[44:47], a[120:123], a[24:27], v[44:47]\n"
    "v_exp_f32 v35, v35\n"
    "v_mfma_f32_16x16x32_bf16 v[44:47], a[124:127], a[28:31], v[44:47]\n"
    "ds_read_b64_tr_b16 a[162:163], v116 offset:45056\n"
    "v_cvt_pk_bf16_f32 v69, v34, v35\n"
    "v_mfma_f32_16x16x32_bf16 v[160:163], a[128:131], v[72:75], v[160:163]\n"
    "v_exp_f32 v36, v36\n"
    "v_mfma_f32_16x16x32_bf16 v[164:167], a[132:135], v[72:75], v[164:167]\n"
    "s_add_u32 m0, %[m0w], 81920\n"
    "ds_read_b64_tr_b16 a[164:165], v117 offset:40960\n"
    "v_mfma_f32_16x16x32_bf16 v[168:171], a[136:139], v[72:75], v[168:171]\n"
    "v_exp_f32 v37, v37\n"
    "v_mfma_f32_16x16x32_bf16 v[172:175], a[140:143], v[72:75], v[172:175]\n"
    "ds_read_b64_tr_b16 a[166:167], v117 offset:45056\n"
    "v_cvt_pk_bf16_f32 v70, v36, v37\n"
    "v_mfma_f32_16x16x32_bf16 v[176:179], a[144:147], v[72:75], v[176:179]\n"
    "v_exp_f32 v38, v38\n"
    "v_mfma_f32_16x16x32_bf16 v[180:183], a[148:151], v[72:75], v[180:183]\n"
    "ds_read_b64_tr_b16 a[168:169], v118 offset:40960\n"
    "v_mfma_f32_16x16x32_bf16 v[184:187], a[152:155], v[72:75], v[184:187]\n"
    "v_exp_f32 v39, v39\n"
    "v_mfma_f32_16x16x32_bf16 v[188:191], a[156:159], v[72:75], v[188:191]\n"
    "ds_read_b64_tr_b16 a[170:171], v118 offset:45056\n"
    "v_cvt_pk_bf16_f32 v71, v38, v39\n"
    "v_mfma_f32_16x16x32_bf16 v[48:51], a[96:99], a[32:35], v[104:107]\n"
    "v_exp_f32 v40, v40\n"
    "v_mfma_f32_16x16x32_bf16 v[48:51], a[100:103], a[36:39], v[48:51]\n"
    "ds_read_b64_tr_b16 a[172:173], v119 offset:40960\n"
    "v_mfma_f32_16x16x32_bf16 v[48:51], a[104:107], a[40:43], v[48:51]\n"
    "v_exp_f32 v41, v41\n"
    "v_mfma_f32_16x16x32_bf16 v[48:51], a[108:111], a[44:47], v[48:51]\n"
    "ds_read_b64_tr_b16 a[174:175], v119 offset:45056\n"
    "v_cvt_pk_bf16_f32 v76, v40, v41\n"
    "v_mfma_f32_16x16x32_bf16 v[52:55], a[112:115], a[32:35], v[104:107]\n"
    "v_exp_f32 v42, v42\n"
    "v_mfma_f32_16x16x32_bf16 v[52:55], a[116:119], a[36:39], v[52:55]\n"
    "buffer_load_dwordx4 %[voffv], %[srdv], s80 offen nt lds\n"
    "v_mfma_f32_16x16x32_bf16 v[52:55], a[120:123], a[40:43], v[52:55]\n"
    "v_exp_f32 v43, v43\n"
    "v_mfma_f32_16x16x32_bf16 v[52:55], a[124:127], a[44:47], v[52:55]\n"
    "ds_read_b64_tr_b16 a[176:177], v120 offset:40960\n"
    "v_cvt_pk_bf16_f32 v77, v42, v43\n"
    "v_mfma_f32_16x16x32_bf16 v[192:195], a[128:131], v[80:83], v[192:195]\n"
    "v_exp_f32 v44, v44\n"
    "v_mfma_f32_16x16x32_bf16 v[196:199], a[132:135], v[80:83], v[196:199]\n"
    "ds_read_b64_tr_b16 a[178:179], v120 offset:45056\n"
    "v_mfma_f32_16x16x32_bf16 v[200:203], a[136:139], v[80:83], v[200:203]\n"
    "v_exp_f32 v45, v45\n"
    "v_mfma_f32_16x16x32_bf16 v[204:207], a[140:143], v[80:83], v[204:207]\n"
    "ds_read_b64_tr_b16 a[180:181], v121 offset:40960\n"
    "v_cvt_pk_bf16_f32 v78, v44, v45\n"
    "v_mfma_f32_16x16x32_bf16 v[208:211], a[144:147], v[80:83], v[208:211]\n"
    "v_exp_f32 v46, v46\n"
    "v_mfma_f32_16x16x32_bf16 v[212:215], a[148:151], v[80:83], v[212:215]\n"
    "ds_read_b64_tr_b16 a[182:183], v121 offset:45056\n"
    "v_mfma_f32_16x16x32_bf16 v[216:219], a[152:155], v[80:83], v[216:219]\n"
    "v_exp_f32 v47, v47\n"
    "v_mfma_f32_16x16x32_bf16 v[220:223], a[156:159], v[80:83], v[220:223]\n"
    "ds_read_b64_tr_b16 a[184:185], v122 offset:40960\n"
    "v_cvt_pk_bf16_f32 v79, v46, v47\n"
    "v_mfma_f32_16x16x32_bf16 v[56:59], a[96:99], a[48:51], v[108:111]\n"
    "v_exp_f32 v48, v48\n"
    "v_mfma_f32_16x16x32_bf16 v[56:59], a[100:103], a[52:55], v[56:59]\n"
    "ds_read_b64_tr_b16 a[186:187], v122 offset:45056\n"
    "v_mfma_f32_16x16x32_bf16 v[56:59], a[104:107], a[56:59], v[56:59]\n"
    "v_exp_f32 v49, v49\n"
    "v_mfma_f32_16x16x32_bf16 v[56:59], a[108:111], a[60:63], v[56:59]\n"
    "ds_read_b64_tr_b16 a[188:189], v123 offset:40960\n"
    "v_cvt_pk_bf16_f32 v84, v48, v49\n"
    "v_mfma_f32_16x16x32_bf16 v[60:63], a[112:115], a[48:51], v[108:111]\n"
    "v_exp_f32 v50, v50\n"
    "v_mfma_f32_16x16x32_bf16 v[60:63], a[116:119], a[52:55], v[60:63]\n"
    "buffer_load_dwordx4 v125, %[srdv], s80 offen offset:1024 nt lds\n"
    "v_mfma_f32_16x16x32_bf16 v[60:63], a[120:123], a[56:59], v[60:63]\n"
    "v_exp_f32 v51, v51\n"
    "v_mfma_f32_16x16x32_bf16 v[60:63], a[124:127], a[60:63], v[60:63]\n"
    "ds_read_b64_tr_b16 a[190:191], v123 offset:45056\n"
    "v_cvt_pk_bf16_f32 v85, v50, v51\n"
    "v_mfma_f32_16x16x32_bf16 v[224:227], a[128:131], v[88:91], v[224:227]\n"
    "v_exp_f32 v52, v52\n"
    "v_mfma_f32_16x16x32_bf16 v[228:231], a[132:135], v[88:91], v[228:231]\n"
    "s_add_u32 s80, s80, 65536\n"
    "v_mfma_f32_16x16x32_bf16 v[232:235], a[136:139], v[88:91], v[232:235]\n"
    "v_exp_f32 v53, v53\n"
    "v_mfma_f32_16x16x32_bf16 v[236:239], a[140:143], v[88:91], v[236:239]\n"
    "v_cvt_pk_bf16_f32 v86, v52, v53\n"
    "v_mfma_f32_16x16x32_bf16 v[240:243], a[144:147], v[88:91], v[240:243]\n"
    "v_exp_f32 v54, v54\n"
    "v_mfma_f32_16x16x32_bf16 v[244:247], a[148:151], v[88:91], v[244:247]\n"
    "v_mfma_f32_16x16x32_bf16 v[248:251], a[152:155], v[88:91], v[248:251]\n"
    "v_exp_f32 v55, v55\n"
    "v_mfma_f32_16x16x32_bf16 v[252:255], a[156:159], v[88:91], v[252:255]\n"
    "v_cvt_pk_bf16_f32 v87, v54, v55\n"
    "; ---- unit 6\n"
    "s_waitcnt lgkmcnt(0)\n"
    "v_mfma_f32_16x16x32_bf16 v[32:35], a[64:67], a[0:3], v[96:99]\n"
    "v_exp_f32 v56, v56\n"
    "v_mfma_f32_16x16x32_bf16 v[32:35], a[68:71], a[4:7], v[32:35]\n"
    "s_waitcnt vmcnt(12)\n"
    "s_barrier\n"
    "s_add_u32 m0, %[m0w], 32768\n"
    "ds_read_b128 a[96:99], v112 offset:57344\n"
    "v_mfma_f32_16x16x32_bf16 v[32:35], a[72:75], a[8:11], v[32:35]\n"
    "v_exp_f32 v57, v57\n"
    "v_mfma_f32_16x16x32_bf16 v[32:35], a[76:79], a[12:15], v[32:35]\n"
    "ds_read_b128 a[100:103], v113 offset:57344\n"
    "v_cvt_pk_bf16_f32 v92, v56, v57\n"
    "v_mfma_f32_16x16x32_bf16 v[36:39], a[80:83], a[0:3], v[96:99]\n"
    "v_exp_f32 v58, v58\n"
    "v_mfma_f32_16x16x32_bf16 v[36:39], a[84:87], a[4:7], v[36:39]\n"
    "buffer_load_dwordx4 %[voffk], %[srdk], s80 offen nt lds\n"
    "v_mfma_f32_16x16x32_bf16 v[36:39], a[88:91], a[8:11], v[36:39]\n"
    "v_exp_f32 v59, v59\n"
    "v_mfma_f32_16x16x32_bf16 v[36:39], a[92:95], a[12:15], v[36:39]\n"
    "ds_read_b128 a[104:107], v114 offset:57344\n"
    "v_cvt_pk_bf16_f32 v93, v58, v59\n"
    "v_mfma_f32_16x16x32_bf16 v[128:131], a[160:163], v[68:71], v[128:131]\n"
    "v_exp_f32 v60, v60\n"
    "v_mfma_f32_16x16x32_bf16 v[132:135], a[164:167], v[68:71], v[132:135]\n"
    "ds_read_b128 a[108:111], v115 offset:57344\n"
    "v_mfma_f32_16x16x32_bf16 v[136:139], a[168:171], v[68:71], v[136:139]\n"
    "v_exp_f32 v61, v61\n"
    "v_mfma_f32_16x16x32_bf16 v[140:143], a[172:175], v[68:71], v[140:143]\n"
    "ds_read_b128 a[112:115], v112 offset:61440\n"
    "v_cvt_pk_bf16_f32 v94, v60, v61\n"
    "v_mfma_f32_16x16x32_bf16 v[144:147], a[176:179], v[68:71], v[144:147]\n"
    "v_exp_f32 v62, v62\n"
    "v_mfma_f32_16x16x32_bf16 v[148:151], a[180:183], v[68:71], v[148:151]\n"
    "ds_read_b128 a[116:119], v113 offset:61440\n"
    "v_mfma_f32_16x16x32_bf16 v[152:155], a[184:187], v[68:71], v[152:155]\n"
    "v_exp_f32 v63, v63\n"
    "v_mfma_f32_16x16x32_bf16 v[156:159], a[188:191], v[68:71], v[156:159]\n"
    "ds_read_b128 a[120:123], v114 offset:61440\n"
    "v_cvt_pk_bf16_f32 v95, v62, v63\n"
    "v_smfmac_f32_16x16x64_bf16 a[192:195], a[208:211], v[64:71], v126\n"
    "v_mfma_f32_16x16x32_bf16 v[40:43], a[64:67], a[16:19], v[100:103]\n"
    "v_exp_f32 v32, v32\n"
    "v_mfma_f32_16x16x32_bf16 v[40:43], a[68:71], a[20:23], v[40:43]\n"
    "ds_read_b128 a[124:127], v115 offset:61440\n"
    "v_mfma_f32_16x16x32_bf16 v[40:43], a[72:75], a[24:27], v[40:43]\n"
    "v_exp_f32 v33, v33\n"
    "v_mfma_f32_16x16x32_bf16 v[40:43], a[76:79], a[28:31], v[40:43]\n"
    "ds_read_b64_tr_b16 a[128:129], v116 offset:49152\n"
    "v_cvt_pk_bf16_f32 v64, v32, v33\n"
    "v_mfma_f32_16x16x32_bf16 v[44:47], a[80:83], a[16:19], v[100:103]\n"
    "v_exp_f32 v34, v34\n"
    "v_mfma_f32_16x16x32_bf16 v[44:47], a[84:87], a[20:23], v[44:47]\n"
    "buffer_load_dwordx4 v124, %[srdk], s80 offen offset:1024 nt lds\n"
    "v_mfma_f32_16x16x32_bf16 v[44:47], a[88:91], a[24:27], v[44:47]\n"
    "v_exp_f32 v35, v35\n"
    "v_mfma_f32_16x16x32_bf16 v[44:47], a[92:95], a[28:31], v[44:47]\n"
    "ds_read_b64_tr_b16 a[130:131], v116 offset:53248\n"
    "v_cvt_pk_bf16_f32 v65, v34, v35\n"
    "v_mfma_f32_16x16x32_bf16 v[160:163], a[160:163], v[76:79], v[160:163]\n"
    "v_exp_f32 v36, v36\n"
    "v_mfma_f32_16x16x32_bf16 v[164:167], a[164:167], v[76:79], v[164:167]\n"
    "s_add_u32 m0, %[m0w], 90112\n"
    "ds_read_b64_tr_b16 a[132:133], v117 offset:49152\n"
    "v_mfma_f32_16x16x32_bf16 v[168:171], a[168:171], v[76:79], v[168:171]\n"
    "v_exp_f32 v37, v37\n"
    "v_mfma_f32_16x16x32_bf16 v[172:175], a[172:175], v[76:79], v[172:175]\n"
    "ds_read_b64_tr_b16 a[134:135], v117 offset:53248\n"
    "v_cvt_pk_bf16_f32 v66, v36, v37\n"
    "v_mfma_f32_16x16x32_bf16 v[176:179], a[176:179], v[76:79], v[176:179]\n"
    "v_exp_f32 v38, v38\n"
    "v_mfma_f32_16x16x32_bf16 v[180:183], a[180:183], v[76:79], v[180:183]\n"
    "ds_read_b64_tr_b16 a[136:137], v118 offset:49152\n"
    "v_mfma_f32_16x16x32_bf16 v[184:187], a[184:187], v[76:79], v[184:187]\n"
    "v_exp_f32 v39, v39\n"
    "v_mfma_f32_16x16x32_bf16 v[188:191], a[188:191], v[76:79], v[188:191]\n"
    "ds_read_b64_tr_b16 a[138:139], v118 offset:53248\n"
    "v_cvt_pk_bf16_f32 v67, v38, v39\n"
    "v_smfmac_f32_16x16x64_bf16 a[196:199], a[208:211], v[72:79], v126\n"
    "v_mfma_f32_16x16x32_bf16 v[48:51], a[64:67], a[32:35], v[104:107]\n"
    "v_exp_f32 v40, v40\n"
    "v_mfma_f32_16x16x32_bf16 v[48:51], a[68:71], a[36:39], v[48:51]\n"
    "ds_read_b64_tr_b16 a[140:141], v119 offset:49152\n"
    "v_mfma_f32_16x16x32_bf16 v[48:51], a[72:75], a[40:43], v[48:51]\n"
    "v_exp_f32 v41, v41\n"
    "v_mfma_f32_16x16x32_bf16 v[48:51], a[76:79], a[44:47], v[48:51]\n"
    "ds_read_b64_tr_b16 a[142:143], v119 offset:53248\n"
    "v_cvt_pk_bf16_f32 v72, v40, v41\n"
    "v_mfma_f32_16x16x32_bf16 v[52:55], a[80:83], a[32:35], v[104:107]\n"
    "v_exp_f32 v42, v42\n"
    "v_mfma_f32_16x16x32_bf16 v[52:55], a[84:87], a[36:39], v[52:55]\n"
    "buffer_load_dwordx4 %[voffv], %[srdv], s80 offen nt lds\n"
    "v_mfma_f32_16x16x32_bf16 v[52:55], a[88:91], a[40:43], v[52:55]\n"
    "v_exp_f32 v43, v43\n"
    "v_mfma_f32_16x16x32_bf16 v[52:55], a[92:95], a[44:47], v[52:55]\n"
    "ds_read_b64_tr_b16 a[144:145], v120 offset:49152\n"
    "v_cvt_pk_bf16_f32 v73, v42, v43\n"
    "v_mfma_f32_16x16x32_bf16 v[192:195], a[160:163], v[84:87], v[192:195]\n"
    "v_exp_f32 v44, v44\n"
    "v_mfma_f32_16x16x32_bf16 v[196:199], a[164:167], v[84:87], v[196:199]\n"
    "ds_read_b64_tr_b16 a[146:147], v120 offset:53248\n"
    "v_mfma_f32_16x16x32_bf16 v[200:203], a[168:171], v[84:87], v[200:203]\n"
    "v_exp_f32 v45, v45\n"
    "v_mfma_f32_16x16x32_bf16 v[204:207], a[172:175], v[84:87], v[204:207]\n"
    "ds_read_b64_tr_b16 a[148:149], v121 offset:49152\n"
    "v_cvt_pk_bf16_f32 v74, v44, v45\n"
    "v_mfma_f32_16x16x32_bf16 v[208:211], a[176:179], v[84:87], v[208:211]\n"
    "v_exp_f32 v46, v46\n"
    "v_mfma_f32_16x16x32_bf16 v[212:215], a[180:183], v[84:87], v[212:215]\n"
    "ds_read_b64_tr_b16 a[150:151], v121 offset:53248\n"
    "v_mfma_f32_16x16x32_bf16 v[216:219], a[184:187], v[84:87], v[216:219]\n"
    "v_exp_f32 v47, v47\n"
    "v_mfma_f32_16x16x32_bf16 v[220:223], a[188:191], v[84:87], v[220:223]\n"
    "ds_read_b64_tr_b16 a[152:153], v122 offset:49152\n"
    "v_cvt_pk_bf16_f32 v75, v46, v47\n"
    "v_smfmac_f32_16x16x64_bf16 a[200:203], a[208:211], v[80:87], v126\n"
    "v_mfma_f32_16x16x32_bf16 v[56:59], a[64:67], a[48:51], v[108:111]\n"
    "v_exp_f32 v48, v48\n"
    "v_mfma_f32_16x16x32_bf16 v[56:59], a[68:71], a[52:55], v[56:59]\n"
    "ds_read_b64_tr_b16 a[154:155], v122 offset:53248\n"
    "v_mfma_f32_16x16x32_bf16 v[56:59], a[72:75], a[56:59], v[56:59]\n"
    "v_exp_f32 v49, v49\n"
    "v_mfma_f32_16x16x32_bf16 v[56:59], a[76:79], a[60:63], v[56:59]\n"
    "ds_read_b64_tr_b16 a[156:157], v123 offset:49152\n"
    "v_cvt_pk_bf16_f32 v80, v48, v49\n"
    "v_mfma_f32_16x16x32_bf16 v[60:63], a[80:83], a[48:51], v[108:111]\n"
    "v_exp_f32 v50, v50\n"
    "v_mfma_f32_16x16x32_bf16 v[60:63], a[84:87], a[52:55], v[60:63]\n"
    "buffer_load_dwordx4 v125, %[srdv], s80 offen offset:1024 nt lds\n"
    "v_mfma_f32_16x16x32_bf16 v[60:63], a[88:91], a[56:59], v[60:63]\n"
    "v_exp_f32 v51, v51\n"
    "v_mfma_f32_16x16x32_bf16 v[60:63], a[92:95], a[60:63], v[60:63]\n"
    "ds_read_b64_tr_b16 a[158:159], v123 offset:53248\n"
    "v_cvt_pk_bf16_f32 v81, v50, v51\n"
    "v_mfma_f32_16x16x32_bf16 v[224:227], a[160:163], v[92:95], v[224:227]\n"
    "v_exp_f32 v52, v52\n"
    "v_mfma_f32_16x16x32_bf16 v[228:231], a[164:167], v[92:95], v[228:231]\n"
    "s_add_u32 s80, s80, 65536\n"
    "v_mfma_f32_16x16x32_bf16 v[232:235], a[168:171], v[92:95], v[232:235]\n"
    "v_exp_f32 v53, v53\n"
    "v_mfma_f32_16x16x32_bf16 v[236:239], a[172:175], v[92:95], v[236:239]\n"
    "v_cvt_pk_bf16_f32 v82, v52, v53\n"
    "v_mfma_f32_16x16x32_bf16 v[240:243], a[176:179], v[92:95], v[240:243]\n"
    "v_exp_f32 v54, v54\n"
    "v_mfma_f32_16x16x32_bf16 v[244:247], a[180:183], v[92:95], v[244:247]\n"
    "v_mfma_f32_16x16x32_bf16 v[248:251], a[184:187], v[92:95], v[248:251]\n"
    "v_exp_f32 v55, v55\n"
    "v_mfma_f32_16x16x32_bf16 v[252:255], a[188:191], v[92:95], v[252:255]\n"
    "v_cvt_pk_bf16_f32 v83, v54, v55\n"
    "v_smfmac_f32_16x16x64_bf16 a[204:207], a[208:211], v[88:95], v126\n"
    "; ---- unit 7\n"
    "s_waitcnt lgkmcnt(0)\n"
    "v_mfma_f32_16x16x32_bf16 v[32:35], a[96:99], a[0:3], v[96:99]\n"
    "v_exp_f32 v56, v56\n"
    "v_mfma_f32_16x16x32_bf16 v[32:35], a[100:103], a[4:7], v[32:35]\n"
    "s_add_u32 m0, %[m0w], 40960\n"
    "ds_read_b128 a[64:67], v112 offset:0\n"
    "v_mfma_f32_16x16x32_bf16 v[32:35], a[104:107], a[8:11], v[32:35]\n"
    "v_exp_f32 v57, v57\n"
    "v_mfma_f32_16x16x32_bf16 v[32:35], a[108:111], a[12:15], v[32:35]\n"
    "ds_read_b128 a[68:71], v113 offset:0\n"
    "v_cvt_pk_bf16_f32 v88, v56, v57\n"
    "v_mfma_f32_16x16x32_bf16 v[36:39], a[112:115], a[0:3], v[96:99]\n"
    "v_exp_f32 v58, v58\n"
    "v_mfma_f32_16x16x32_bf16 v[36:39], a[116:119], a[4:7], v[36:39]\n"
    "buffer_load_dwordx4 %[voffk], %[srdk], s80 offen nt lds\n"
    "v_mfma_f32_16x16x32_bf16 v[36:39], a[120:123], a[8:11], v[36:39]\n"
    "v_exp_f32 v59, v59\n"
    "v_mfma_f32_16x16x32_bf16 v[36:39], a[124:127], a[12:15], v[36:39]\n"
    "ds_read_b128 a[72:75], v114 offset:0\n"
    "v_cvt_pk_bf16_f32 v89, v58, v59\n"
    "v_mfma_f32_16x16x32_bf16 v[128:131], a[128:131], v[64:67], v[128:131]\n"
    "v_exp_f32 v60, v60\n"
    "v_mfma_f32_16x16x32_bf16 v[132:135], a[132:135], v[64:67], v[132:135]\n"
    "ds_read_b128 a[76:79], v115 offset:0\n"
    "v_mfma_f32_16x16x32_bf16 v[136:139], a[136:139], v[64:67], v[136:139]\n"
    "v_exp_f32 v61, v61\n"
    "v_mfma_f32_16x16x32_bf16 v[140:143], a[140:143], v[64:67], v[140:143]\n"
    "ds_read_b128 a[80:83], v112 offset:4096\n"
    "v_cvt_pk_bf16_f32 v90, v60, v61\n"
    "v_mfma_f32_16x16x32_bf16 v[144:147], a[144:147], v[64:67], v[144:147]\n"
    "v_exp_f32 v62, v62\n"
    "v_mfma_f32_16x16x32_bf16 v[148:151], a[148:151], v[64:67], v[148:151]\n"
    "ds_read_b128 a[84:87], v113 offset:4096\n"
    "v_mfma_f32_16x16x32_bf16 v[152:155], a[152:155], v[64:67], v[152:155]\n"
    "v_exp_f32 v63, v63\n"
    "v_mfma_f32_16x16x32_bf16 v[156:159], a[156:159], v[64:67], v[156:159]\n"
    "ds_read_b128 a[88:91], v114 offset:4096\n"
    "v_cvt_pk_bf16_f32 v91, v62, v63\n"
    "v_mfma_f32_16x16x32_bf16 v[40:43], a[96:99], a[16:19], v[100:103]\n"
    "v_exp_f32 v32, v32\n"
    "v_mfma_f32_16x16x32_bf16 v[40:43], a[100:103], a[20:23], v[40:43]\n"
    "ds_read_b128 a[92:95], v115 offset:4096\n"
    "v_mfma_f32_16x16x32_bf16 v[40:43], a[104:107], a[24:27], v[40:43]\n"
    "v_exp_f32 v33, v33\n"
    "v_mfma_f32_16x16x32_bf16 v[40:43], a[108:111], a[28:31], v[40:43]\n"
    "ds_read_b64_tr_b16 a[160:161], v116 offset:57344\n"
    "v_cvt_pk_bf16_f32 v68, v32, v33\n"
    "v_mfma_f32_16x16x32_bf16 v[44:47], a[112:115], a[16:19], v[100:103]\n"
    "v_exp_f32 v34, v34\n"
    "v_mfma_f32_16x16x32_bf16 v[44:47], a[116:119], a[20:23], v[44:47]\n"
    "buffer_load_dwordx4 v124, %[srdk], s80 offen offset:1024 nt lds\n"
    "v_mfma_f32_16x16x32_bf16 v[44:47], a[120:123], a[24:27], v[44:47]\n"
    "v_exp_f32 v35, v35\n"
    "v_mfma_f32_16x16x32_bf16 v[44:47], a[124:127], a[28:31], v[44:47]\n"
    "ds_read_b64_tr_b16 a[162:163], v116 offset:61440\n"
    "v_cvt_pk_bf16_f32 v69, v34, v35\n"
    "v_mfma_f32_16x16x32_bf16 v[160:163], a[128:131], v[72:75], v[160:163]\n"
    "v_exp_f32 v36, v36\n"
    "v_mfma_f32_16x16x32_bf16 v[164:167], a[132:135], v[72:75], v[164:167]\n"
    "s_add_u32 m0, %[m0w], 98304\n"
    "ds_read_b64_tr_b16 a[164:165], v117 offset:57344\n"
    "v_mfma_f32_16x16x32_bf16 v[168:171], a[136:139], v[72:75], v[168:171]\n"
    "v_exp_f32 v37, v37\n"
    "v_mfma_f32_16x16x32_bf16 v[172:175], a[140:143], v[72:75], v[172:175]\n"
    "ds_read_b64_tr_b16 a[166:167], v117 offset:61440\n"
    "v_cvt_pk_bf16_f32 v70, v36, v37\n"
    "v_mfma_f32_16x16x32_bf16 v[176:179], a[144:147], v[72:75], v[176:179]\n"
    "v_exp_f32 v38, v38\n"
    "v_mfma_f32_16x16x32_bf16 v[180:183], a[148:151], v[72:75], v[180:183]\n"
    "ds_read_b64_tr_b16 a[168:169], v118 offset:57344\n"
    "v_mfma_f32_16x16x32_bf16 v[184:187], a[152:155], v[72:75], v[184:187]\n"
    "v_exp_f32 v39, v39\n"
    "v_mfma_f32_16x16x32_bf16 v[188:191], a[156:159], v[72:75], v[188:191]\n"
    "ds_read_b64_tr_b16 a[170:171], v118 offset:61440\n"
    "v_cvt_pk_bf16_f32 v71, v38, v39\n"
    "v_mfma_f32_16x16x32_bf16 v[48:51], a[96:99], a[32:35], v[104:107]\n"
    "v_exp_f32 v40, v40\n"
    "v_mfma_f32_16x16x32_bf16 v[48:51], a[100:103], a[36:39], v[48:51]\n"
    "ds_read_b64_tr_b16 a[172:173], v119 offset:57344\n"
    "v_mfma_f32_16x16x32_bf16 v[48:51], a[104:107], a[40:43], v[48:51]\n"
    "v_exp_f32 v41, v41\n"
    "v_mfma_f32_16x16x32_bf16 v[48:51], a[108:111], a[44:47], v[48:51]\n"
    "ds_read_b64_tr_b16 a[174:175], v119 offset:61440\n"
    "v_cvt_pk_bf16_f32 v76, v40, v41\n"
    "v_mfma_f32_16x16x32_bf16 v[52:55], a[112:115], a[32:35], v[104:107]\n"
    "v_exp_f32 v42, v42\n"
    "v_mfma_f32_16x16x32_bf16 v[52:55], a[116:119], a[36:39], v[52:55]\n"
    "buffer_load_dwordx4 %[voffv], %[srdv], s80 offen nt lds\n"
    "v_mfma_f32_16x16x32_bf16 v[52:55], a[120:123], a[40:43], v[52:55]\n"
    "v_exp_f32 v43, v43\n"
    "v_mfma_f32_16x16x32_bf16 v[52:55], a[124:127], a[44:47], v[52:55]\n"
    "ds_read_b64_tr_b16 a[176:177], v120 offset:57344\n"
    "v_cvt_pk_bf16_f32 v77, v42, v43\n"
    "v_mfma_f32_16x16x32_bf16 v[192:195], a[128:131], v[80:83], v[192:195]\n"
    "v_exp_f32 v44, v44\n"
    "v_mfma_f32_16x16x32_bf16 v[196:199], a[132:135], v[80:83], v[196:199]\n"
    "ds_read_b64_tr_b16 a[178:179], v120 offset:61440\n"
    "v_mfma_f32_16x16x32_bf16 v[200:203], a[136:139], v[80:83], v[200:203]\n"
    "v_exp_f32 v45, v45\n"
    "v_mfma_f32_16x16x32_bf16 v[204:207], a[140:143], v[80:83], v[204:207]\n"
    "ds_read_b64_tr_b16 a[180:181], v121 offset:57344\n"
    "v_cvt_pk_bf16_f32 v78, v44, v45\n"
    "v_mfma_f32_16x16x32_bf16 v[208:211], a[144:147], v[80:83], v[208:211]\n"
    "v_exp_f32 v46, v46\n"
    "v_mfma_f32_16x16x32_bf16 v[212:215], a[148:151], v[80:83], v[212:215]\n"
    "ds_read_b64_tr_b16 a[182:183], v121 offset:61440\n"
    "v_mfma_f32_16x16x32_bf16 v[216:219], a[152:155], v[80:83], v[216:219]\n"
    "v_exp_f32 v47, v47\n"
    "v_mfma_f32_16x16x32_bf16 v[220:223], a[156:159], v[80:83], v[220:223]\n"
    "ds_read_b64_tr_b16 a[184:185], v122 offset:57344\n"
    "v_cvt_pk_bf16_f32 v79, v46, v47\n"
    "v_mfma_f32_16x16x32_bf16 v[56:59], a[96:99], a[48:51], v[108:111]\n"
    "v_exp_f32 v48, v48\n"
    "v_mfma_f32_16x16x32_bf16 v[56:59], a[100:103], a[52:55], v[56:59]\n"
    "ds_read_b64_tr_b16 a[186:187], v122 offset:61440\n"
    "v_mfma_f32_16x16x32_bf16 v[56:59], a[104:107], a[56:59], v[56:59]\n"
    "v_exp_f32 v49, v49\n"
    "v_mfma_f32_16x16x32_bf16 v[56:59], a[108:111], a[60:63], v[56:59]\n"
    "ds_read_b64_tr_b16 a[188:189], v123 offset:57344\n"
    "v_cvt_pk_bf16_f32 v84, v48, v49\n"
    "v_mfma_f32_16x16x32_bf16 v[60:63], a[112:115], a[48:51], v[108:111]\n"
    "v_exp_f32 v50, v50\n"
    "v_mfma_f32_16x16x32_bf16 v[60:63], a[116:119], a[52:55], v[60:63]\n"
    "buffer_load_dwordx4 v125, %[srdv], s80 offen offset:1024 nt lds\n"
    "v_mfma_f32_16x16x32_bf16 v[60:63], a[120:123], a[56:59], v[60:63]\n"
    "v_exp_f32 v51, v51\n"
    "v_mfma_f32_16x16x32_bf16 v[60:63], a[124:127], a[60:63], v[60:63]\n"
    "ds_read_b64_tr_b16 a[190:191], v123 offset:61440\n"
    "v_cvt_pk_bf16_f32 v85, v50, v51\n"
    "v_mfma_f32_16x16x32_bf16 v[224:227], a[128:131], v[88:91], v[224:227]\n"
    "v_exp_f32 v52, v52\n"
    "v_mfma_f32_16x16x32_bf16 v[228:231], a[132:135], v[88:91], v[228:231]\n"
    "s_add_u32 s80, s80, 65536\n"
    "s_cmp_eq_u32 s80, 1966080\n"
    "v_mfma_f32_16x16x32_bf16 v[232:235], a[136:139], v[88:91], v[232:235]\n"
    "v_exp_f32 v53, v53\n"
    "v_mfma_f32_16x16x32_bf16 v[236:239], a[140:143], v[88:91], v[236:239]\n"
    "v_cvt_pk_bf16_f32 v86, v52, v53\n"
    "v_mfma_f32_16x16x32_bf16 v[240:243], a[144:147], v[88:91], v[240:243]\n"
    "v_exp_f32 v54, v54\n"
    "v_mfma_f32_16x16x32_bf16 v[244:247], a[148:151], v[88:91], v[244:247]\n"
    "v_mfma_f32_16x16x32_bf16 v[248:251], a[152:155], v[88:91], v[248:251]\n"
    "v_exp_f32 v55, v55\n"
    "v_mfma_f32_16x16x32_bf16 v[252:255], a[156:159], v[88:91], v[252:255]\n"
    "v_cvt_pk_bf16_f32 v87, v54, v55\n"
    "s_cbranch_scc1 Ltail%=\n"
    "; ---- unit 8\n"
    "s_waitcnt lgkmcnt(0)\n"
    "v_mfma_f32_16x16x32_bf16 v[32:35], a[64:67], a[0:3], v[96:99]\n"
    "Lh5_%=:\n"
    "s_cbranch_execz Lr5_%=\n"
    "v_exp_f32 v56, v56\n"
    "v_mfma_f32_16x16x32_bf16 v[32:35], a[68:71], a[4:7], v[32:35]\n"
    "s_waitcnt vmcnt(12)\n"
    "s_barrier\n"
    "s_add_u32 m0, %[m0w], 49152\n"
    "ds_read_b128 a[96:99], v112 offset:8192\n"
    "v_mfma_f32_16x16x32_bf16 v[32:35], a[72:75], a[8:11], v[32:35]\n"
    "v_exp_f32 v57, v57\n"
    "v_mfma_f32_16x16x32_bf16 v[32:35], a[76:79], a[12:15], v[32:35]\n"
    "ds_read_b128 a[100:103], v113 offset:8192\n"
    "v_cvt_pk_bf16_f32 v92, v56, v57\n"
    "v_mfma_f32_16x16x32_bf16 v[36:39], a[80:83], a[0:3], v[96:99]\n"
    "v_exp_f32 v58, v58\n"
    "v_mfma_f32_16x16x32_bf16 v[36:39], a[84:87], a[4:7], v[36:39]\n"
    "buffer_load_dwordx4 %[voffk], %[srdk], s80 offen nt lds\n"
    "v_mfma_f32_16x16x32_bf16 v[36:39], a[88:91], a[8:11], v[36:39]\n"
    "v_exp_f32 v59, v59\n"
    "v_mfma_f32_16x16x32_bf16 v[36:39], a[92:95], a[12:15], v[36:39]\n"
    "ds_read_b128 a[104:107], v114 offset:8192\n"
    "v_cvt_pk_bf16_f32 v93, v58, v59\n"
    "v_mfma_f32_16x16x32_bf16 v[128:131], a[160:163], v[68:71], v[128:131]\n"
    "v_exp_f32 v60, v60\n"
    "v_mfma_f32_16x16x32_bf16 v[132:135], a[164:167], v[68:71], v[132:135]\n"
    "ds_read_b128 a[108:111], v115 offset:8192\n"
    "v_mfma_f32_16x16x32_bf16 v[136:139], a[168:171], v[68:71], v[136:139]\n"
    "v_exp_f32 v61, v61\n"
    "v_mfma_f32_16x16x32_bf16 v[140:143], a[172:175], v[68:71], v[140:143]\n"
    "ds_read_b128 a[112:115], v112 offset:12288\n"
    "v_cvt_pk_bf16_f32 v94, v60, v61\n"
    "v_mfma_f32_16x16x32_bf16 v[144:147], a[176:179], v[68:71], v[144:147]\n"
    "v_exp_f32 v62, v62\n"
    "v_mfma_f32_16x16x32_bf16 v[148:151], a[180:183], v[68:71], v[148:151]\n"
    "ds_read_b128 a[116:119], v113 offset:12288\n"
    "v_mfma_f32_16x16x32_bf16 v[152:155], a[184:187], v[68:71], v[152:155]\n"
    "v_exp_f32 v63, v63\n"
    "v_mfma_f32_16x16x32_bf16 v[156:159], a[188:191], v[68:71], v[156:159]\n"
    "ds_read_b128 a[120:123], v114 offset:12288\n"
    "v_cvt_pk_bf16_f32 v95, v62, v63\n"
    "v_smfmac_f32_16x16x64_bf16 a[192:195], a[208:211], v[64:71], v126\n"
    "v_mfma_f32_16x16x32_bf16 v[40:43], a[64:67], a[16:19], v[100:103]\n"
    "v_exp_f32 v32, v32\n"
    "v_mfma_f32_16x16x32_bf16 v[40:43], a[68:71], a[20:23], v[40:43]\n"
    "ds_read_b128 a[124:127], v115 offset:12288\n"
    "v_mfma_f32_16x16x32_bf16 v[40:43], a[72:75], a[24:27], v[40:43]\n"
    "v_exp_f32 v33, v33\n"
    "v_mfma_f32_16x16x32_bf16 v[40:43], a[76:79], a[28:31], v[40:43]\n"
    "ds_read_b64_tr_b16 a[128:129], v116 offset:0\n"
    "v_cvt_pk_bf16_f32 v64, v32, v33\n"
    "v_mfma_f32_16x16x32_bf16 v[44:47], a[80:83], a[16:19], v[100:103]\n"
    "v_exp_f32 v34, v34\n"
    "v_mfma_f32_16x16x32_bf16 v[44:47], a[84:87], a[20:23], v[44:47]\n"
    "buffer_load_dwordx4 v124, %[srdk], s80 offen offset:1024 nt lds\n"
    "v_mfma_f32_16x16x32_bf16 v[44:47], a[88:91], a[24:27], v[44:47]\n"
    "v_exp_f32 v35, v35\n"
    "v_mfma_f32_16x16x32_bf16 v[44:47], a[92:95], a[28:31], v[44:47]\n"
    "ds_read_b64_tr_b16 a[130:131], v116 offset:4096\n"
    "v_cvt_pk_bf16_f32 v65, v34, v35\n"
    "v_mfma_f32_16x16x32_bf16 v[160:163], a[160:163], v[76:79], v[160:163]\n"
    "v_exp_f32 v36, v36\n"
    "v_mfma_f32_16x16x32_bf16 v[164:167], a[164:167], v[76:79], v[164:167]\n"
    "s_add_u32 m0, %[m0w], 106496\n"
    "ds_read_b64_tr_b16 a[132:133], v117 offset:0\n"
    "v_mfma_f32_16x16x32_bf16 v[168:171], a[168:171], v[76:79], v[168:171]\n"
    "v_exp_f32 v37, v37\n"
    "v_mfma_f32_16x16x32_bf16 v[172:175], a[172:175], v[76:79], v[172:175]\n"
    "ds_read_b64_tr_b16 a[134:135], v117 offset:4096\n"
    "v_cvt_pk_bf16_f32 v66, v36, v37\n"
    "v_mfma_f32_16x16x32_bf16 v[176:179], a[176:179], v[76:79], v[176:179]\n"
    "v_exp_f32 v38, v38\n"
    "v_mfma_f32_16x16x32_bf16 v[180:183], a[180:183], v[76:79], v[180:183]\n"
    "ds_read_b64_tr_b16 a[136:137], v118 offset:0\n"
    "v_mfma_f32_16x16x32_bf16 v[184:187], a[184:187], v[76:79], v[184:187]\n"
    "v_exp_f32 v39, v39\n"
    "v_mfma_f32_16x16x32_bf16 v[188:191], a[188:191], v[76:79], v[188:191]\n"
    "ds_read_b64_tr_b16 a[138:139], v118 offset:4096\n"
    "v_cvt_pk_bf16_f32 v67, v38, v39\n"
    "v_smfmac_f32_16x16x64_bf16 a[196:199], a[208:211], v[72:79], v126\n"
    "v_mfma_f32_16x16x32_bf16 v[48:51], a[64:67], a[32:35], v[104:107]\n"
    "v_exp_f32 v40, v40\n"
    "v_mfma_f32_16x16x32_bf16 v[48:51], a[68:71], a[36:39], v[48:51]\n"
    "ds_read_b64_tr_b16 a[140:141], v119 offset:0\n"
    "v_mfma_f32_16x16x32_bf16 v[48:51], a[72:75], a[40:43], v[48:51]\n"
    "v_exp_f32 v41, v41\n"
    "v_mfma_f32_16x16x32_bf16 v[48:51], a[76:79], a[44:47], v[48:51]\n"
    "ds_read_b64_tr_b16 a[142:143], v119 offset:4096\n"
    "v_cvt_pk_bf16_f32 v72, v40, v41\n"
    "v_mfma_f32_16x16x32_bf16 v[52:55], a[80:83], a[32:35], v[104:107]\n"
    "v_exp_f32 v42, v42\n"
    "v_mfma_f32_16x16x32_bf16 v[52:55], a[84:87], a[36:39], v[52:55]\n"
    "buffer_load_dwordx4 %[voffv], %[srdv], s80 offen nt lds\n"
    "v_mfma_f32_16x16x32_bf16 v[52:55], a[88:91], a[40:43], v[52:55]\n"
    "v_exp_f32 v43, v43\n"
    "v_mfma_f32_16x16x32_bf16 v[52:55], a[92:95], a[44:47], v[52:55]\n"
    "ds_read_b64_tr_b16 a[144:145], v120 offset:0\n"
    "v_cvt_pk_bf16_f32 v73, v42, v43\n"
    "v_mfma_f32_16x16x32_bf16 v[192:195], a[160:163], v[84:87], v[192:195]\n"
    "v_exp_f32 v44, v44\n"
    "v_mfma_f32_16x16x32_bf16 v[196:199], a[164:167], v[84:87], v[196:199]\n"
    "ds_read_b64_tr_b16 a[146:147], v120 offset:4096\n"
    "v_mfma_f32_16x16x32_bf16 v[200:203], a[168:171], v[84:87], v[200:203]\n"
    "v_exp_f32 v45, v45\n"
    "v_mfma_f32_16x16x32_bf16 v[204:207], a[172:175], v[84:87], v[204:207]\n"
    "ds_read_b64_tr_b16 a[148:149], v121 offset:0\n"
    "v_cvt_pk_bf16_f32 v74, v44, v45\n"
    "v_mfma_f32_16x16x32_bf16 v[208:211], a[176:179], v[84:87], v[208:211]\n"
    "v_exp_f32 v46, v46\n"
    "v_mfma_f32_16x16x32_bf16 v[212:215], a[180:183], v[84:87], v[212:215]\n"
    "ds_read_b64_tr_b16 a[150:151], v121 offset:4096\n"
    "v_mfma_f32_16x16x32_bf16 v[216:219], a[184:187], v[84:87], v[216:219]\n"
    "v_exp_f32 v47, v47\n"
    "v_mfma_f32_16x16x32_bf16 v[220:223], a[188:191], v[84:87], v[220:223]\n"
    "ds_read_b64_tr_b16 a[152:153], v122 offset:0\n"
    "v_cvt_pk_bf16_f32 v75, v46, v47\n"
    "v_smfmac_f32_16x16x64_bf16 a[200:203], a[208:211], v[80:87], v126\n"
    "v_mfma_f32_16x16x32_bf16 v[56:59], a[64:67], a[48:51], v[108:111]\n"
    "v_exp_f32 v48, v48\n"
    "v_mfma_f32_16x16x32_bf16 v[56:59], a[68:71], a[52:55], v[56:59]\n"
    "ds_read_b64_tr_b16 a[154:155], v122 offset:4096\n"
    "v_mfma_f32_16x16x32_bf16 v[56:59], a[72:75], a[56:59], v[56:59]\n"
    "v_exp_f32 v49, v49\n"
    "v_mfma_f32_16x16x32_bf16 v[56:59], a[76:79], a[60:63], v[56:59]\n"
    "ds_read_b64_tr_b16 a[156:157], v123 offset:0\n"
    "v_cvt_pk_bf16_f32 v80, v48, v49\n"
    "v_mfma_f32_16x16x32_bf16 v[60:63], a[80:83], a[48:51], v[108:111]\n"
    "v_exp_f32 v50, v50\n"
    "v_mfma_f32_16x16x32_bf16 v[60:63], a[84:87], a[52:55], v[60:63]\n"
    "buffer_load_dwordx4 v125, %[srdv], s80 offen offset:1024 nt lds\n"
    "v_mfma_f32_16x16x32_bf16 v[60:63], a[88:91], a[56:59], v[60:63]\n"
    "v_exp_f32 v51, v51\n"
    "v_mfma_f32_16x16x32_bf16 v[60:63], a[92:95], a[60:63], v[60:63]\n"
    "ds_read_b64_tr_b16 a[158:159], v123 offset:4096\n"
    "v_cvt_pk_bf16_f32 v81, v50, v51\n"
    "v_mfma_f32_16x16x32_bf16 v[224:227], a[160:163], v[92:95], v[224:227]\n"
    "v_exp_f32 v52, v52\n"
    "v_mfma_f32_16x16x32_bf16 v[228:231], a[164:167], v[92:95], v[228:231]\n"
    "s_add_u32 s80, s80, 65536\n"
    "v_mfma_f32_16x16x32_bf16 v[232:235], a[168:171], v[92:95], v[232:235]\n"
    "v_exp_f32 v53, v53\n"
    "v_mfma_f32_16x16x32_bf16 v[236:239], a[172:175], v[92:95], v[236:239]\n"
    "v_cvt_pk_bf16_f32 v82, v52, v53\n"
    "v_mfma_f32_16x16x32_bf16 v[240:243], a[176:179], v[92:95], v[240:243]\n"
    "v_exp_f32 v54, v54\n"
    "v_mfma_f32_16x16x32_bf16 v[244:247], a[180:183], v[92:95], v[244:247]\n"
    "v_mfma_f32_16x16x32_bf16 v[248:251], a[184:187], v[92:95], v[248:251]\n"
    "v_exp_f32 v55, v55\n"
    "v_mfma_f32_16x16x32_bf16 v[252:255], a[188:191], v[92:95], v[252:255]\n"
    "v_cvt_pk_bf16_f32 v83, v54, v55\n"
    "v_smfmac_f32_16x16x64_bf16 a[204:207], a[208:211], v[88:95], v126\n"
    "; ---- unit 9\n"
    "s_waitcnt lgkmcnt(0)\n"
    "v_mfma_f32_16x16x32_bf16 v[32:35], a[96:99], a[0:3], v[96:99]\n"
    "v_exp_f32 v56, v56\n"
    "v_mfma_f32_16x16x32_bf16 v[32:35], a[100:103], a[4:7], v[32:35]\n"
    "s_add_u32 m0, %[m0w], 57344\n"
    "ds_read_b128 a[64:67], v112 offset:16384\n"
    "v_mfma_f32_16x16x32_bf16 v[32:35], a[104:107], a[8:11], v[32:35]\n"
    "v_exp_f32 v57, v57\n"
    "v_mfma_f32_16x16x32_bf16 v[32:35], a[108:111], a[12:15], v[32:35]\n"
    "ds_read_b128 a[68:71], v113 offset:16384\n"
    "v_cvt_pk_bf16_f32 v88, v56, v57\n"
    "v_mfma_f32_16x16x32_bf16 v[36:39], a[112:115], a[0:3], v[96:99]\n"
    "v_exp_f32 v58, v58\n"
    "v_mfma_f32_16x16x32_bf16 v[36:39], a[116:119], a[4:7], v[36:39]\n"
    "buffer_load_dwordx4 %[voffk], %[srdk], s80 offen nt lds\n"
    "v_mfma_f32_16x16x32_bf16 v[36:39], a[120:123], a[8:11], v[36:39]\n"
    "v_exp_f32 v59, v59\n"
    "v_mfma_f32_16x16x32_bf16 v[36:39], a[124:127], a[12:15], v[36:39]\n"
    "ds_read_b128 a[72:75], v114 offset:16384\n"
    "v_cvt_pk_bf16_f32 v89, v58, v59\n"
    "v_mfma_f32_16x16x32_bf16 v[128:131], a[128:131], v[64:67], v[128:131]\n"
    "v_exp_f32 v60, v60\n"
    "v_mfma_f32_16x16x32_bf16 v[132:135], a[132:135], v[64:67], v[132:135]\n"
    "ds_read_b128 a[76:79], v115 offset:16384\n"
    "v_mfma_f32_16x16x32_bf16 v[136:139], a[136:139], v[64:67], v[136:139]\n"
    "v_exp_f32 v61, v61\n"
    "v_mfma_f32_16x16x32_bf16 v[140:143], a[140:143], v[64:67], v[140:143]\n"
    "ds_read_b128 a[80:83], v112 offset:20480\n"
    "v_cvt_pk_bf16_f32 v90, v60, v61\n"
    "v_mfma_f32_16x16x32_bf16 v[144:147], a[144:147], v[64:67], v[144:147]\n"
    "v_exp_f32 v62, v62\n"
    "v_mfma_f32_16x16x32_bf16 v[148:151], a[148:151], v[64:67], v[148:151]\n"
    "ds_read_b128 a[84:87], v113 offset:20480\n"
    "v_mfma_f32_16x16x32_bf16 v[152:155], a[152:155], v[64:67], v[152:155]\n"
    "v_exp_f32 v63, v63\n"
    "v_mfma_f32_16x16x32_bf16 v[156:159], a[156:159], v[64:67], v[156:159]\n"
    "ds_read_b128 a[88:91], v114 offset:20480\n"
    "v_cvt_pk_bf16_f32 v91, v62, v63\n"
    "v_mfma_f32_16x16x32_bf16 v[40:43], a[96:99], a[16:19], v[100:103]\n"
    "v_exp_f32 v32, v32\n"
    "v_mfma_f32_16x16x32_bf16 v[40:43], a[100:103], a[20:23], v[40:43]\n"
    "ds_read_b128 a[92:95], v115 offset:20480\n"
    "v_mfma_f32_16x16x32_bf16 v[40:43], a[104:107], a[24:27], v[40:43]\n"
    "v_exp_f32 v33, v33\n"
    "v_mfma_f32_16x16x32_bf16 v[40:43], a[108:111], a[28:31], v[40:43]\n"
    "ds_read_b64_tr_b16 a[160:161], v116 offset:8192\n"
    "v_cvt_pk_bf16_f32 v68, v32, v33\n"
    "v_mfma_f32_16x16x32_bf16 v[44:47], a[112:115], a[16:19], v[100:103]\n"
    "v_exp_f32 v34, v34\n"
    "v_mfma_f32_16x16x32_bf16 v[44:47], a[116:119], a[20:23], v[44:47]\n"
    "buffer_load_dwordx4 v124, %[srdk], s80 offen offset:1024 nt lds\n"
    "v_mfma_f32_16x16x32_bf16 v[44:47], a[120:123], a[24:27], v[44:47]\n"
    "v_exp_f32 v35, v35\n"
    "v_mfma_f32_16x16x32_bf16 v[44:47], a[124:127], a[28:31], v[44:47]\n"
    "ds_read_b64_tr_b16 a[162:163], v116 offset:12288\n"
    "v_cvt_pk_bf16_f32 v69, v34, v35\n"
    "v_mfma_f32_16x16x32_bf16 v[160:163], a[128:131], v[72:75], v[160:163]\n"
    "v_exp_f32 v36, v36\n"
    "v_mfma_f32_16x16x32_bf16 v[164:167], a[132:135], v[72:75], v[164:167]\n"
    "s_add_u32 m0, %[m0w], 114688\n"
    "ds_read_b64_tr_b16 a[164:165], v117 offset:8192\n"
    "v_mfma_f32_16x16x32_bf16 v[168:171], a[136:139], v[72:75], v[168:171]\n"
    "v_exp_f32 v37, v37\n"
    "v_mfma_f32_16x16x32_bf16 v[172:175], a[140:143], v[72:75], v[172:175]\n"
    "ds_read_b64_tr_b16 a[166:167], v117 offset:12288\n"
    "v_cvt_pk_bf16_f32 v70, v36, v37\n"
    "v_mfma_f32_16x16x32_bf16 v[176:179], a[144:147], v[72:75], v[176:179]\n"
    "v_exp_f32 v38, v38\n"
    "v_mfma_f32_16x16x32_bf16 v[180:183], a[148:151], v[72:75], v[180:183]\n"
    "ds_read_b64_tr_b16 a[168:169], v118 offset:8192\n"
    "v_mfma_f32_16x16x32_bf16 v[184:187], a[152:155], v[72:75], v[184:187]\n"
    "v_exp_f32 v39, v39\n"
    "v_mfma_f32_16x16x32_bf16 v[188:191], a[156:159], v[72:75], v[188:191]\n"
    "ds_read_b64_tr_b16 a[170:171], v118 offset:12288\n"
    "v_cvt_pk_bf16_f32 v71, v38, v39\n"
    "v_mfma_f32_16x16x32_bf16 v[48:51], a[96:99], a[32:35], v[104:107]\n"
    "v_exp_f32 v40, v40\n"
    "v_mfma_f32_16x16x32_bf16 v[48:51], a[100:103], a[36:39], v[48:51]\n"
    "ds_read_b64_tr_b16 a[172:173], v119 offset:8192\n"
    "v_mfma_f32_16x16x32_bf16 v[48:51], a[104:107], a[40:43], v[48:51]\n"
    "v_exp_f32 v41, v41\n"
    "v_mfma_f32_16x16x32_bf16 v[48:51], a[108:111], a[44:47], v[48:51]\n"
    "ds_read_b64_tr_b16 a[174:175], v119 offset:12288\n"
    "v_cvt_pk_bf16_f32 v76, v40, v41\n"
    "v_mfma_f32_16x16x32_bf16 v[52:55], a[112:115], a[32:35], v[104:107]\n"
    "v_exp_f32 v42, v42\n"
    "v_mfma_f32_16x16x32_bf16 v[52:55], a[116:119], a[36:39], v[52:55]\n"
    "buffer_load_dwordx4 %[voffv], %[srdv], s80 offen nt lds\n"
    "v_mfma_f32_16x16x32_bf16 v[52:55], a[120:123], a[40:43], v[52:55]\n"
    "v_exp_f32 v43, v43\n"
    "v_mfma_f32_16x16x32_bf16 v[52:55], a[124:127], a[44:47], v[52:55]\n"
    "ds_read_b64_tr_b16 a[176:177], v120 offset:8192\n"
    "v_cvt_pk_bf16_f32 v77, v42, v43\n"
    "v_mfma_f32_16x16x32_bf16 v[192:195], a[128:131], v[80:83], v[192:195]\n"
    "v_exp_f32 v44, v44\n"
    "v_mfma_f32_16x16x32_bf16 v[196:199], a[132:135], v[80:83], v[196:199]\n"
    "ds_read_b64_tr_b16 a[178:179], v120 offset:12288\n"
    "v_mfma_f32_16x16x32_bf16 v[200:203], a[136:139], v[80:83], v[200:203]\n"
    "v_exp_f32 v45, v45\n"
    "v_mfma_f32_16x16x32_bf16 v[204:207], a[140:143], v[80:83], v[204:207]\n"
    "ds_read_b64_tr_b16 a[180:181], v121 offset:8192\n"
    "v_cvt_pk_bf16_f32 v78, v44, v45\n"
    "v_mfma_f32_16x16x32_bf16 v[208:211], a[144:147], v[80:83], v[208:211]\n"
    "v_exp_f32 v46, v46\n"
    "v_mfma_f32_16x16x32_bf16 v[212:215], a[148:151], v[80:83], v[212:215]\n"
    "ds_read_b64_tr_b16 a[182:183], v121 offset:12288\n"
    "v_mfma_f32_16x16x32_bf16 v[216:219], a[152:155], v[80:83], v[216:219]\n"
    "v_exp_f32 v47, v47\n"
    "v_mfma_f32_16x16x32_bf16 v[220:223], a[156:159], v[80:83], v[220:223]\n"
    "ds_read_b64_tr_b16 a[184:185], v122 offset:8192\n"
    "v_cvt_pk_bf16_f32 v79, v46, v47\n"
    "v_mfma_f32_16x16x32_bf16 v[56:59], a[96:99], a[48:51], v[108:111]\n"
    "v_exp_f32 v48, v48\n"
    "v_mfma_f32_16x16x32_bf16 v[56:59], a[100:103], a[52:55], v[56:59]\n"
    "ds_read_b64_tr_b16 a[186:187], v122 offset:12288\n"
    "v_mfma_f32_16x16x32_bf16 v[56:59], a[104:107], a[56:59], v[56:59]\n"
    "v_exp_f32 v49, v49\n"
    "v_mfma_f32_16x16x32_bf16 v[56:59], a[108:111], a[60:63], v[56:59]\n"
    "ds_read_b64_tr_b16 a[188:189], v123 offset:8192\n"
    "v_cvt_pk_bf16_f32 v84, v48, v49\n"
    "v_mfma_f32_16x16x32_bf16 v[60:63], a[112:115], a[48:51], v[108:111]\n"
    "v_exp_f32 v50, v50\n"
    "v_mfma_f32_16x16x32_bf16 v[60:63], a[116:119], a[52:55], v[60:63]\n"
    "buffer_load_dwordx4 v125, %[srdv], s80 offen offset:1024 nt lds\n"
    "v_mfma_f32_16x16x32_bf16 v[60:63], a[120:123], a[56:59], v[60:63]\n"
    "v_exp_f32 v51, v51\n"
    "v_mfma_f32_16x16x32_bf16 v[60:63], a[124:127], a[60:63], v[60:63]\n"
    "ds_read_b64_tr_b16 a[190:191], v123 offset:12288\n"
    "v_cvt_pk_bf16_f32 v85, v50, v51\n"
    "v_mfma_f32_16x16x32_bf16 v[224:227], a[128:131], v[88:91], v[224:227]\n"
    "v_exp_f32 v52, v52\n"
    "v_mfma_f32_16x16x32_bf16 v[228:231], a[132:135], v[88:91], v[228:231]\n"
    "s_add_u32 s80, s80, 65536\n"
    "v_mfma_f32_16x16x32_bf16 v[232:235], a[136:139], v[88:91], v[232:235]\n"
    "v_exp_f32 v53, v53\n"
    "v_mfma_f32_16x16x32_bf16 v[236:239], a[140:143], v[88:91], v[236:239]\n"
    "v_cvt_pk_bf16_f32 v86, v52, v53\n"
    "v_mfma_f32_16x16x32_bf16 v[240:243], a[144:147], v[88:91], v[240:243]\n"
    "v_exp_f32 v54, v54\n"
    "v_mfma_f32_16x16x32_bf16 v[244:247], a[148:151], v[88:91], v[244:247]\n"
    "v_mfma_f32_16x16x32_bf16 v[248:251], a[152:155], v[88:91], v[248:251]\n"
    "v_exp_f32 v55, v55\n"
    "v_mfma_f32_16x16x32_bf16 v[252:255], a[156:159], v[88:91], v[252:255]\n"
    "v_cvt_pk_bf16_f32 v87, v54, v55\n"
    "; ---- unit 10\n"
    "s_waitcnt lgkmcnt(0)\n"
    "v_mfma_f32_16x16x32_bf16 v[32:35], a[64:67], a[0:3], v[96:99]\n"
    "v_exp_f32 v56, v56\n"
    "v_mfma_f32_16x16x32_bf16 v[32:35], a[68:71], a[4:7], v[32:35]\n"
    "s_waitcnt vmcnt(12)\n"
    "s_barrier\n"
    "s_add_u32 m0, %[m0w], 0\n"
    "ds_read_b128 a[96:99], v112 offset:24576\n"
    "v_mfma_f32_16x16x32_bf16 v[32:35], a[72:75], a[8:11], v[32:35]\n"
    "v_exp_f32 v57, v57\n"
    "v_mfma_f32_16x16x32_bf16 v[32:35], a[76:79], a[12:15], v[32:35]\n"
    "ds_read_b128 a[100:103], v113 offset:24576\n"
    "v_cvt_pk_bf16_f32 v92, v56, v57\n"
    "v_mfma_f32_16x16x32_bf16 v[36:39], a[80:83], a[0:3], v[96:99]\n"
    "v_exp_f32 v58, v58\n"
    "v_mfma_f32_16x16x32_bf16 v[36:39], a[84:87], a[4:7], v[36:39]\n"
    "buffer_load_dwordx4 %[voffk], %[srdk], s80 offen nt lds\n"
    "v_mfma_f32_16x16x32_bf16 v[36:39], a[88:91], a[8:11], v[36:39]\n"
    "v_exp_f32 v59, v59\n"
    "v_mfma_f32_16x16x32_bf16 v[36:39], a[92:95], a[12:15], v[36:39]\n"
    "ds_read_b128 a[104:107], v114 offset:24576\n"
    "v_cvt_pk_bf16_f32 v93, v58, v59\n"
    "v_mfma_f32_16x16x32_bf16 v[128:131], a[160:163], v[68:71], v[128:131]\n"
    "v_exp_f32 v60, v60\n"
    "v_mfma_f32_16x16x32_bf16 v[132:135], a[164:167], v[68:71], v[132:135]\n"
    "ds_read_b128 a[108:111], v115 offset:24576\n"
    "v_mfma_f32_16x16x32_bf16 v[136:139], a[168:171], v[68:71], v[136:139]\n"
    "v_exp_f32 v61, v61\n"
    "v_mfma_f32_16x16x32_bf16 v[140:143], a[172:175], v[68:71], v[140:143]\n"
    "ds_read_b128 a[112:115], v112 offset:28672\n"
    "v_cvt_pk_bf16_f32 v94, v60, v61\n"
    "v_mfma_f32_16x16x32_bf16 v[144:147], a[176:179], v[68:71], v[144:147]\n"
    "v_exp_f32 v62, v62\n"
    "v_mfma_f32_16x16x32_bf16 v[148:151], a[180:183], v[68:71], v[148:151]\n"
    "ds_read_b128 a[116:119], v113 offset:28672\n"
    "v_mfma_f32_16x16x32_bf16 v[152:155], a[184:187], v[68:71], v[152:155]\n"
    "v_exp_f32 v63, v63\n"
    "v_mfma_f32_16x16x32_bf16 v[156:159], a[188:191], v[68:71], v[156:159]\n"
    "ds_read_b128 a[120:123], v114 offset:28672\n"
    "v_cvt_pk_bf16_f32 v95, v62, v63\n"
    "v_smfmac_f32_16x16x64_bf16 a[192:195], a[208:211], v[64:71], v126\n"
    "v_mfma_f32_16x16x32_bf16 v[40:43], a[64:67], a[16:19], v[100:103]\n"
    "v_exp_f32 v32, v32\n"
    "v_mfma_f32_16x16x32_bf16 v[40:43], a[68:71], a[20:23], v[40:43]\n"
    "ds_read_b128 a[124:127], v115 offset:28672\n"
    "v_mfma_f32_16x16x32_bf16 v[40:43], a[72:75], a[24:27], v[40:43]\n"
    "v_exp_f32 v33, v33\n"
    "v_mfma_f32_16x16x32_bf16 v[40:43], a[76:79], a[28:31], v[40:43]\n"
    "ds_read_b64_tr_b16 a[128:129], v116 offset:16384\n"
    "v_cvt_pk_bf16_f32 v64, v32, v33\n"
    "v_mfma_f32_16x16x32_bf16 v[44:47], a[80:83], a[16:19], v[100:103]\n"
    "v_exp_f32 v34, v34\n"
    "v_mfma_f32_16x16x32_bf16 v[44:47], a[84:87], a[20:23], v[44:47]\n"
    "buffer_load_dwordx4 v124, %[srdk], s80 offen offset:1024 nt lds\n"
    "v_mfma_f32_16x16x32_bf16 v[44:47], a[88:91], a[24:27], v[44:47]\n"
    "v_exp_f32 v35, v35\n"
    "v_mfma_f32_16x16x32_bf16 v[44:47], a[92:95], a[28:31], v[44:47]\n"
    "ds_read_b64_tr_b16 a[130:131], v116 offset:20480\n"
    "v_cvt_pk_bf16_f32 v65, v34, v35\n"
    "v_mfma_f32_16x16x32_bf16 v[160:163], a[160:163], v[76:79], v[160:163]\n"
    "v_exp_f32 v36, v36\n"
    "v_mfma_f32_16x16x32_bf16 v[164:167], a[164:167], v[76:79], v[164:167]\n"
    "s_add_u32 m0, %[m0w], 122880\n"
    "ds_read_b64_tr_b16 a[132:133], v117 offset:16384\n"
    "v_mfma_f32_16x16x32_bf16 v[168:171], a[168:171], v[76:79], v[168:171]\n"
    "v_exp_f32 v37, v37\n"
    "v_mfma_f32_16x16x32_bf16 v[172:175], a[172:175], v[76:79], v[172:175]\n"
    "ds_read_b64_tr_b16 a[134:135], v117 offset:20480\n"
    "v_cvt_pk_bf16_f32 v66, v36, v37\n"
    "v_mfma_f32_16x16x32_bf16 v[176:179], a[176:179], v[76:79], v[176:179]\n"
    "v_exp_f32 v38, v38\n"
    "v_mfma_f32_16x16x32_bf16 v[180:183], a[180:183], v[76:79], v[180:183]\n"
    "ds_read_b64_tr_b16 a[136:137], v118 offset:16384\n"
    "v_mfma_f32_16x16x32_bf16 v[184:187], a[184:187], v[76:79], v[184:187]\n"
    "v_exp_f32 v39, v39\n"
    "v_mfma_f32_16x16x32_bf16 v[188:191], a[188:191], v[76:79], v[188:191]\n"
    "ds_read_b64_tr_b16 a[138:139], v118 offset:20480\n"
    "v_cvt_pk_bf16_f32 v67, v38, v39\n"
    "v_smfmac_f32_16x16x64_bf16 a[196:199], a[208:211], v[72:79], v126\n"
    "v_mfma_f32_16x16x32_bf16 v[48:51], a[64:67], a[32:35], v[104:107]\n"
    "v_exp_f32 v40, v40\n"
    "v_mfma_f32_16x16x32_bf16 v[48:51], a[68:71], a[36:39], v[48:51]\n"
    "ds_read_b64_tr_b16 a[140:141], v119 offset:16384\n"
    "v_mfma_f32_16x16x32_bf16 v[48:51], a[72:75], a[40:43], v[48:51]\n"
    "v_exp_f32 v41, v41\n"
    "v_mfma_f32_16x16x32_bf16 v[48:51], a[76:79], a[44:47], v[48:51]\n"
    "ds_read_b64_tr_b16 a[142:143], v119 offset:20480\n"
    "v_cvt_pk_bf16_f32 v72, v40, v41\n"
    "v_mfma_f32_16x16x32_bf16 v[52:55], a[80:83], a[32:35], v[104:107]\n"
    "v_exp_f32 v42, v42\n"
    "v_mfma_f32_16x16x32_bf16 v[52:55], a[84:87], a[36:39], v[52:55]\n"
    "buffer_load_dwordx4 %[voffv], %[srdv], s80 offen nt lds\n"
    "v_mfma_f32_16x16x32_bf16 v[52:55], a[88:91], a[40:43], v[52:55]\n"
    "v_exp_f32 v43, v43\n"
    "v_mfma_f32_16x16x32_bf16 v[52:55], a[92:95], a[44:47], v[52:55]\n"
    "ds_read_b64_tr_b16 a[144:145], v120 offset:16384\n"
    "v_cvt_pk_bf16_f32 v73, v42, v43\n"
    "v_mfma_f32_16x16x32_bf16 v[192:195], a[160:163], v[84:87], v[192:195]\n"
    "v_exp_f32 v44, v44\n"
    "v_mfma_f32_16x16x32_bf16 v[196:199], a[164:167], v[84:87], v[196:199]\n"
    "ds_read_b64_tr_b16 a[146:147], v120 offset:20480\n"
    "v_mfma_f32_16x16x32_bf16 v[200:203], a[168:171], v[84:87], v[200:203]\n"
    "v_exp_f32 v45, v45\n"
    "v_mfma_f32_16x16x32_bf16 v[204:207], a[172:175], v[84:87], v[204:207]\n"
    "ds_read_b64_tr_b16 a[148:149], v121 offset:16384\n"
    "v_cvt_pk_bf16_f32 v74, v44, v45\n"
    "v_mfma_f32_16x16x32_bf16 v[208:211], a[176:179], v[84:87], v[208:211]\n"
    "v_exp_f32 v46, v46\n"
    "v_mfma_f32_16x16x32_bf16 v[212:215], a[180:183], v[84:87], v[212:215]\n"
    "ds_read_b64_tr_b16 a[150:151], v121 offset:20480\n"
    "v_mfma_f32_16x16x32_bf16 v[216:219], a[184:187], v[84:87], v[216:219]\n"
    "v_exp_f32 v47, v47\n"
    "v_mfma_f32_16x16x32_bf16 v[220:223], a[188:191], v[84:87], v[220:223]\n"
    "ds_read_b64_tr_b16 a[152:153], v122 offset:16384\n"
    "v_cvt_pk_bf16_f32 v75, v46, v47\n"
    "v_smfmac_f32_16x16x64_bf16 a[200:203], a[208:211], v[80:87], v126\n"
    "v_mfma_f32_16x16x32_bf16 v[56:59], a[64:67], a[48:51], v[108:111]\n"
    "v_exp_f32 v48, v48\n"
    "v_mfma_f32_16x16x32_bf16 v[56:59], a[68:71], a[52:55], v[56:59]\n"
    "ds_read_b64_tr_b16 a[154:155], v122 offset:20480\n"
    "v_mfma_f32_16x16x32_bf16 v[56:59], a[72:75], a[56:59], v[56:59]\n"
    "v_exp_f32 v49, v49\n"
    "v_mfma_f32_16x16x32_bf16 v[56:59], a[76:79], a[60:63], v[56:59]\n"
    "ds_read_b64_tr_b16 a[156:157], v123 offset:16384\n"
    "v_cvt_pk_bf16_f32 v80, v48, v49\n"
    "v_mfma_f32_16x16x32_bf16 v[60:63], a[80:83], a[48:51], v[108:111]\n"
    "v_exp_f32 v50, v50\n"
    "v_mfma_f32_16x16x32_bf16 v[60:63], a[84:87], a[52:55], v[60:63]\n"
    "buffer_load_dwordx4 v125, %[srdv], s80 offen offset:1024 nt lds\n"
    "v_mfma_f32_16x16x32_bf16 v[60:63], a[88:91], a[56:59], v[60:63]\n"
    "v_exp_f32 v51, v51\n"
    "v_mfma_f32_16x16x32_bf16 v[60:63], a[92:95], a[60:63], v[60:63]\n"
    "ds_read_b64_tr_b16 a[158:159], v123 offset:20480\n"
    "v_cvt_pk_bf16_f32 v81, v50, v51\n"
    "v_mfma_f32_16x16x32_bf16 v[224:227], a[160:163], v[92:95], v[224:227]\n"
    "v_exp_f32 v52, v52\n"
    "v_mfma_f32_16x16x32_bf16 v[228:231], a[164:167], v[92:95], v[228:231]\n"
    "s_add_u32 s80, s80, 65536\n"
    "v_mfma_f32_16x16x32_bf16 v[232:235], a[168:171], v[92:95], v[232:235]\n"
    "v_exp_f32 v53, v53\n"
    "v_mfma_f32_16x16x32_bf16 v[236:239], a[172:175], v[92:95], v[236:239]\n"
    "v_cvt_pk_bf16_f32 v82, v52, v53\n"
    "v_mfma_f32_16x16x32_bf16 v[240:243], a[176:179], v[92:95], v[240:243]\n"
    "v_exp_f32 v54, v54\n"
    "v_mfma_f32_16x16x32_bf16 v[244:247], a[180:183], v[92:95], v[244:247]\n"
    "v_mfma_f32_16x16x32_bf16 v[248:251], a[184:187], v[92:95], v[248:251]\n"
    "v_exp_f32 v55, v55\n"
    "v_mfma_f32_16x16x32_bf16 v[252:255], a[188:191], v[92:95], v[252:255]\n"
    "v_cvt_pk_bf16_f32 v83, v54, v55\n"
    "v_smfmac_f32_16x16x64_bf16 a[204:207], a[208:211], v[88:95], v126\n"
    "; ---- unit 11\n"
    "s_waitcnt lgkmcnt(0)\n"
    "v_mfma_f32_16x16x32_bf16 v[32:35], a[96:99], a[0:3], v[96:99]\n"
    "v_exp_f32 v56, v56\n"
    "v_mfma_f32_16x16x32_bf16 v[32:35], a[100:103], a[4:7], v[32:35]\n"
    "s_add_u32 m0, %[m0w], 8192\n"
    "ds_read_b128 a[64:67], v112 offset:32768\n"
    "v_mfma_f32_16x16x32_bf16 v[32:35], a[104:107], a[8:11], v[32:35]\n"
    "v_exp_f32 v57, v57\n"
    "v_mfma_f32_16x16x32_bf16 v[32:35], a[108:111], a[12:15], v[32:35]\n"
    "ds_read_b128 a[68:71], v113 offset:32768\n"
    "v_cvt_pk_bf16_f32 v88, v56, v57\n"
    "v_mfma_f32_16x16x32_bf16 v[36:39], a[112:115], a[0:3], v[96:99]\n"
    "v_exp_f32 v58, v58\n"
    "v_mfma_f32_16x16x32_bf16 v[36:39], a[116:119], a[4:7], v[36:39]\n"
    "buffer_load_dwordx4 %[voffk], %[srdk], s80 offen nt lds\n"
    "v_mfma_f32_16x16x32_bf16 v[36:39], a[120:123], a[8:11], v[36:39]\n"
    "v_exp_f32 v59, v59\n"
    "v_mfma_f32_16x16x32_bf16 v[36:39], a[124:127], a[12:15], v[36:39]\n"
    "ds_read_b128 a[72:75], v114 offset:32768\n"
    "v_cvt_pk_bf16_f32 v89, v58, v59\n"
    "v_mfma_f32_16x16x32_bf16 v[128:131], a[128:131], v[64:67], v[128:131]\n"
    "v_exp_f32 v60, v60\n"
    "v_mfma_f32_16x16x32_bf16 v[132:135], a[132:135], v[64:67], v[132:135]\n"
    "ds_read_b128 a[76:79], v115 offset:32768\n"
    "v_mfma_f32_16x16x32_bf16 v[136:139], a[136:139], v[64:67], v[136:139]\n"
    "v_exp_f32 v61, v61\n"
    "v_mfma_f32_16x16x32_bf16 v[140:143], a[140:143], v[64:67], v[140:143]\n"
    "ds_read_b128 a[80:83], v112 offset:36864\n"
    "v_cvt_pk_bf16_f32 v90, v60, v61\n"
    "v_mfma_f32_16x16x32_bf16 v[144:147], a[144:147], v[64:67], v[144:147]\n"
    "v_exp_f32 v62, v62\n"
    "v_mfma_f32_16x16x32_bf16 v[148:151], a[148:151], v[64:67], v[148:151]\n"
    "ds_read_b128 a[84:87], v113 offset:36864\n"
    "v_mfma_f32_16x16x32_bf16 v[152:155], a[152:155], v[64:67], v[152:155]\n"
    "v_exp_f32 v63, v63\n"
    "v_mfma_f32_16x16x32_bf16 v[156:159], a[156:159], v[64:67], v[156:159]\n"
    "ds_read_b128 a[88:91], v114 offset:36864\n"
    "v_cvt_pk_bf16_f32 v91, v62, v63\n"
    "v_mfma_f32_16x16x32_bf16 v[40:43], a[96:99], a[16:19], v[100:103]\n"
    "v_exp_f32 v32, v32\n"
    "v_mfma_f32_16x16x32_bf16 v[40:43], a[100:103], a[20:23], v[40:43]\n"
    "ds_read_b128 a[92:95], v115 offset:36864\n"
    "v_mfma_f32_16x16x32_bf16 v[40:43], a[104:107], a[24:27], v[40:43]\n"
    "v_exp_f32 v33, v33\n"
    "v_mfma_f32_16x16x32_bf16 v[40:43], a[108:111], a[28:31], v[40:43]\n"
    "ds_read_b64_tr_b16 a[160:161], v116 offset:24576\n"
    "v_cvt_pk_bf16_f32 v68, v32, v33\n"
    "v_mfma_f32_16x16x32_bf16 v[44:47], a[112:115], a[16:19], v[100:103]\n"
    "v_exp_f32 v34, v34\n"
    "v_mfma_f32_16x16x32_bf16 v[44:47], a[116:119], a[20:23], v[44:47]\n"
    "buffer_load_dwordx4 v124, %[srdk], s80 offen offset:1024 nt lds\n"
    "v_mfma_f32_16x16x32_bf16 v[44:47], a[120:123], a[24:27], v[44:47]\n"
    "v_exp_f32 v35, v35\n"
    "v_mfma_f32_16x16x32_bf16 v[44:47], a[124:127], a[28:31], v[44:47]\n"
    "ds_read_b64_tr_b16 a[162:163], v116 offset:28672\n"
    "v_cvt_pk_bf16_f32 v69, v34, v35\n"
    "v_mfma_f32_16x16x32_bf16 v[160:163], a[128:131], v[72:75], v[160:163]\n"
    "v_exp_f32 v36, v36\n"
    "v_mfma_f32_16x16x32_bf16 v[164:167], a[132:135], v[72:75], v[164:167]\n"
    "s_add_u32 m0, %[m0w], 65536\n"
    "ds_read_b64_tr_b16 a[164:165], v117 offset:24576\n"
    "v_mfma_f32_16x16x32_bf16 v[168:171], a[136:139], v[72:75], v[168:171]\n"
    "v_exp_f32 v37, v37\n"
    "v_mfma_f32_16x16x32_bf16 v[172:175], a[140:143], v[72:75], v[172:175]\n"
    "ds_read_b64_tr_b16 a[166:167], v117 offset:28672\n"
    "v_cvt_pk_bf16_f32 v70, v36, v37\n"
    "v_mfma_f32_16x16x32_bf16 v[176:179], a[144:147], v[72:75], v[176:179]\n"
    "v_exp_f32 v38, v38\n"
    "v_mfma_f32_16x16x32_bf16 v[180:183], a[148:151], v[72:75], v[180:183]\n"
    "ds_read_b64_tr_b16 a[168:169], v118 offset:24576\n"
    "v_mfma_f32_16x16x32_bf16 v[184:187], a[152:155], v[72:75], v[184:187]\n"
    "v_exp_f32 v39, v39\n"
    "v_mfma_f32_16x16x32_bf16 v[188:191], a[156:159], v[72:75], v[188:191]\n"
    "ds_read_b64_tr_b16 a[170:171], v118 offset:28672\n"
    "v_cvt_pk_bf16_f32 v71, v38, v39\n"
    "v_mfma_f32_16x16x32_bf16 v[48:51], a[96:99], a[32:35], v[104:107]\n"
    "v_exp_f32 v40, v40\n"
    "v_mfma_f32_16x16x32_bf16 v[48:51], a[100:103], a[36:39], v[48:51]\n"
    "ds_read_b64_tr_b16 a[172:173], v119 offset:24576\n"
    "v_mfma_f32_16x16x32_bf16 v[48:51], a[104:107], a[40:43], v[48:51]\n"
    "v_exp_f32 v41, v41\n"
    "v_mfma_f32_16x16x32_bf16 v[48:51], a[108:111], a[44:47], v[48:51]\n"
    "ds_read_b64_tr_b16 a[174:175], v119 offset:28672\n"
    "v_cvt_pk_bf16_f32 v76, v40, v41\n"
    "v_mfma_f32_16x16x32_bf16 v[52:55], a[112:115], a[32:35], v[104:107]\n"
    "v_exp_f32 v42, v42\n"
    "v_mfma_f32_16x16x32_bf16 v[52:55], a[116:119], a[36:39], v[52:55]\n"
    "buffer_load_dwordx4 %[voffv], %[srdv], s80 offen nt lds\n"
    "v_mfma_f32_16x16x32_bf16 v[52:55], a[120:123], a[40:43], v[52:55]\n"
    "v_exp_f32 v43, v43\n"
    "v_mfma_f32_16x16x32_bf16 v[52:55], a[124:127], a[44:47], v[52:55]\n"
    "ds_read_b64_tr_b16 a[176:177], v120 offset:24576\n"
    "v_cvt_pk_bf16_f32 v77, v42, v43\n"
    "v_mfma_f32_16x16x32_bf16 v[192:195], a[128:131], v[80:83], v[192:195]\n"
    "v_exp_f32 v44, v44\n"
    "v_mfma_f32_16x16x32_bf16 v[196:199], a[132:135], v[80:83], v[196:199]\n"
    "ds_read_b64_tr_b16 a[178:179], v120 offset:28672\n"
    "v_mfma_f32_16x16x32_bf16 v[200:203], a[136:139], v[80:83], v[200:203]\n"
    "v_exp_f32 v45, v45\n"
    "v_mfma_f32_16x16x32_bf16 v[204:207], a[140:143], v[80:83], v[204:207]\n"
    "ds_read_b64_tr_b16 a[180:181], v121 offset:24576\n"
    "v_cvt_pk_bf16_f32 v78, v44, v45\n"
    "v_mfma_f32_16x16x32_bf16 v[208:211], a[144:147], v[80:83], v[208:211]\n"
    "v_exp_f32 v46, v46\n"
    "v_mfma_f32_16x16x32_bf16 v[212:215], a[148:151], v[80:83], v[212:215]\n"
    "ds_read_b64_tr_b16 a[182:183], v121 offset:28672\n"
    "v_mfma_f32_16x16x32_bf16 v[216:219], a[152:155], v[80:83], v[216:219]\n"
    "v_exp_f32 v47, v47\n"
    "v_mfma_f32_16x16x32_bf16 v[220:223], a[156:159], v[80:83], v[220:223]\n"
    "ds_read_b64_tr_b16 a[184:185], v122 offset:24576\n"
    "v_cvt_pk_bf16_f32 v79, v46, v47\n"
    "v_mfma_f32_16x16x32_bf16 v[56:59], a[96:99], a[48:51], v[108:111]\n"
    "v_exp_f32 v48, v48\n"
    "v_mfma_f32_16x16x32_bf16 v[56:59], a[100:103], a[52:55], v[56:59]\n"
    "ds_read_b64_tr_b16 a[186:187], v122 offset:28672\n"
    "v_mfma_f32_16x16x32_bf16 v[56:59], a[104:107], a[56:59], v[56:59]\n"
    "v_exp_f32 v49, v49\n"
    "v_mfma_f32_16x16x32_bf16 v[56:59], a[108:111], a[60:63], v[56:59]\n"
    "ds_read_b64_tr_b16 a[188:189], v123 offset:24576\n"
    "v_cvt_pk_bf16_f32 v84, v48, v49\n"
    "v_mfma_f32_16x16x32_bf16 v[60:63], a[112:115], a[48:51], v[108:111]\n"
    "v_exp_f32 v50, v50\n"
    "v_mfma_f32_16x16x32_bf16 v[60:63], a[116:119], a[52:55], v[60:63]\n"
    "buffer_load_dwordx4 v125, %[srdv], s80 offen offset:1024 nt lds\n"
    "v_mfma_f32_16x16x32_bf16 v[60:63], a[120:123], a[56:59], v[60:63]\n"
    "v_exp_f32 v51, v51\n"
    "v_mfma_f32_16x16x32_bf16 v[60:63], a[124:127], a[60:63], v[60:63]\n"
    "ds_read_b64_tr_b16 a[190:191], v123 offset:28672\n"
    "v_cvt_pk_bf16_f32 v85, v50, v51\n"
    "v_mfma_f32_16x16x32_bf16 v[224:227], a[128:131], v[88:91], v[224:227]\n"
    "v_exp_f32 v52, v52\n"
    "v_mfma_f32_16x16x32_bf16 v[228:231], a[132:135], v[88:91], v[228:231]\n"
    "s_add_u32 s80, s80, 65536\n"
    "v_mfma_f32_16x16x32_bf16 v[232:235], a[136:139], v[88:91], v[232:235]\n"
    "v_exp_f32 v53, v53\n"
    "v_mfma_f32_16x16x32_bf16 v[236:239], a[140:143], v[88:91], v[236:239]\n"
    "v_cvt_pk_bf16_f32 v86, v52, v53\n"
    "v_mfma_f32_16x16x32_bf16 v[240:243], a[144:147], v[88:91], v[240:243]\n"
    "Lh6_%=:\n"
    "s_cbranch_execz Lr6_%=\n"
    "v_exp_f32 v54, v54\n"
    "v_mfma_f32_16x16x32_bf16 v[244:247], a[148:151], v[88:91], v[244:247]\n"
    "v_mfma_f32_16x16x32_bf16 v[248:251], a[152:155], v[88:91], v[248:251]\n"
    "v_exp_f32 v55, v55\n"
    "v_mfma_f32_16x16x32_bf16 v[252:255], a[156:159], v[88:91], v[252:255]\n"
    "v_cvt_pk_bf16_f32 v87, v54, v55\n"
    "; ---- unit 12\n"
    "s_waitcnt lgkmcnt(0)\n"
    "v_mfma_f32_16x16x32_bf16 v[32:35], a[64:67], a[0:3], v[96:99]\n"
    "v_exp_f32 v56, v56\n"
    "v_mfma_f32_16x16x32_bf16 v[32:35], a[68:71], a[4:7], v[32:35]\n"
    "s_waitcnt vmcnt(12)\n"
    "s_barrier\n"
    "s_add_u32 m0, %[m0w], 16384\n"
    "ds_read_b128 a[96:99], v112 offset:40960\n"
    "v_mfma_f32_16x16x32_bf16 v[32:35], a[72:75], a[8:11], v[32:35]\n"
    "v_exp_f32 v57, v57\n"
    "v_mfma_f32_16x16x32_bf16 v[32:35], a[76:79], a[12:15], v[32:35]\n"
    "ds_read_b128 a[100:103], v113 offset:40960\n"
    "v_cvt_pk_bf16_f32 v92, v56, v57\n"
    "v_mfma_f32_16x16x32_bf16 v[36:39], a[80:83], a[0:3], v[96:99]\n"
    "v_exp_f32 v58, v58\n"
    "v_mfma_f32_16x16x32_bf16 v[36:39], a[84:87], a[4:7], v[36:39]\n"
    "buffer_load_dwordx4 %[voffk], %[srdk], s80 offen nt lds\n"
    "v_mfma_f32_16x16x32_bf16 v[36:39], a[88:91], a[8:11], v[36:39]\n"
    "v_exp_f32 v59, v59\n"
    "v_mfma_f32_16x16x32_bf16 v[36:39], a[92:95], a[12:15], v[36:39]\n"
    "ds_read_b128 a[104:107], v114 offset:40960\n"
    "v_cvt_pk_bf16_f32 v93, v58, v59\n"
    "v_mfma_f32_16x16x32_bf16 v[128:131], a[160:163], v[68:71], v[128:131]\n"
    "v_exp_f32 v60, v60\n"
    "v_mfma_f32_16x16x32_bf16 v[132:135], a[164:167], v[68:71], v[132:135]\n"
    "ds_read_b128 a[108:111], v115 offset:40960\n"
    "v_mfma_f32_16x16x32_bf16 v[136:139], a[168:171], v[68:71], v[136:139]\n"
    "v_exp_f32 v61, v61\n"
    "v_mfma_f32_16x16x32_bf16 v[140:143], a[172:175], v[68:71], v[140:143]\n"
    "ds_read_b128 a[112:115], v112 offset:45056\n"
    "v_cvt_pk_bf16_f32 v94, v60, v61\n"
    "v_mfma_f32_16x16x32_bf16 v[144:147], a[176:179], v[68:71], v[144:147]\n"
    "v_exp_f32 v62, v62\n"
    "v_mfma_f32_16x16x32_bf16 v[148:151], a[180:183], v[68:71], v[148:151]\n"
    "ds_read_b128 a[116:119], v113 offset:45056\n"
    "v_mfma_f32_16x16x32_bf16 v[152:155], a[184:187], v[68:71], v[152:155]\n"
    "v_exp_f32 v63, v63\n"
    "v_mfma_f32_16x16x32_bf16 v[156:159], a[188:191], v[68:71], v[156:159]\n"
    "ds_read_b128 a[120:123], v114 offset:45056\n"
    "v_cvt_pk_bf16_f32 v95, v62, v63\n"
    "v_smfmac_f32_16x16x64_bf16 a[192:195], a[208:211], v[64:71], v126\n"
    "v_mfma_f32_16x16x32_bf16 v[40:43], a[64:67], a[16:19], v[100:103]\n"
    "v_exp_f32 v32, v32\n"
    "v_mfma_f32_16x16x32_bf16 v[40:43], a[68:71], a[20:23], v[40:43]\n"
    "ds_read_b128 a[124:127], v115 offset:45056\n"
    "v_mfma_f32_16x16x32_bf16 v[40:43], a[72:75], a[24:27], v[40:43]\n"
    "v_exp_f32 v33, v33\n"
    "v_mfma_f32_16x16x32_bf16 v[40:43], a[76:79], a[28:31], v[40:43]\n"
    "ds_read_b64_tr_b16 a[128:129], v116 offset:32768\n"
    "v_cvt_pk_bf16_f32 v64, v32, v33\n"
    "v_mfma_f32_16x16x32_bf16 v[44:47], a[80:83], a[16:19], v[100:103]\n"
    "v_exp_f32 v34, v34\n"
    "v_mfma_f32_16x16x32_bf16 v[44:47], a[84:87], a[20:23], v[44:47]\n"
    "buffer_load_dwordx4 v124, %[srdk], s80 offen offset:1024 nt lds\n"
    "v_mfma_f32_16x16x32_bf16 v[44:47], a[88:91], a[24:27], v[44:47]\n"
    "v_exp_f32 v35, v35\n"
    "v_mfma_f32_16x16x32_bf16 v[44:47], a[92:95], a[28:31], v[44:47]\n"
    "ds_read_b64_tr_b16 a[130:131], v116 offset:36864\n"
    "v_cvt_pk_bf16_f32 v65, v34, v35\n"
    "v_mfma_f32_16x16x32_bf16 v[160:163], a[160:163], v[76:79], v[160:163]\n"
    "v_exp_f32 v36, v36\n"
    "v_mfma_f32_16x16x32_bf16 v[164:167], a[164:167], v[76:79], v[164:167]\n"
    "s_add_u32 m0, %[m0w], 73728\n"
    "ds_read_b64_tr_b16 a[132:133], v117 offset:32768\n"
    "v_mfma_f32_16x16x32_bf16 v[168:171], a[168:171], v[76:79], v[168:171]\n"
    "v_exp_f32 v37, v37\n"
    "v_mfma_f32_16x16x32_bf16 v[172:175], a[172:175], v[76:79], v[172:175]\n"
    "ds_read_b64_tr_b16 a[134:135], v117 offset:36864\n"
    "v_cvt_pk_bf16_f32 v66, v36, v37\n"
    "v_mfma_f32_16x16x32_bf16 v[176:179], a[176:179], v[76:79], v[176:179]\n"
    "v_exp_f32 v38, v38\n"
    "v_mfma_f32_16x16x32_bf16 v[180:183], a[180:183], v[76:79], v[180:183]\n"
    "ds_read_b64_tr_b16 a[136:137], v118 offset:32768\n"
    "v_mfma_f32_16x16x32_bf16 v[184:187], a[184:187], v[76:79], v[184:187]\n"
    "v_exp_f32 v39, v39\n"
    "v_mfma_f32_16x16x32_bf16 v[188:191], a[188:191], v[76:79], v[188:191]\n"
    "ds_read_b64_tr_b16 a[138:139], v118 offset:36864\n"
    "v_cvt_pk_bf16_f32 v67, v38, v39\n"
    "v_smfmac_f32_16x16x64_bf16 a[196:199], a[208:211], v[72:79], v126\n"
    "v_mfma_f32_16x16x32_bf16 v[48:51], a[64:67], a[32:35], v[104:107]\n"
    "v_exp_f32 v40, v40\n"
    "v_mfma_f32_16x16x32_bf16 v[48:51], a[68:71], a[36:39], v[48:51]\n"
    "ds_read_b64_tr_b16 a[140:141], v119 offset:32768\n"
    "v_mfma_f32_16x16x32_bf16 v[48:51], a[72:75], a[40:43], v[48:51]\n"
    "v_exp_f32 v41, v41\n"
    "v_mfma_f32_16x16x32_bf16 v[48:51], a[76:79], a[44:47], v[48:51]\n"
    "ds_read_b64_tr_b16 a[142:143], v119 offset:36864\n"
    "v_cvt_pk_bf16_f32 v72, v40, v41\n"
    "v_mfma_f32_16x16x32_bf16 v[52:55], a[80:83], a[32:35], v[104:107]\n"
    "v_exp_f32 v42, v42\n"
    "v_mfma_f32_16x16x32_bf16 v[52:55], a[84:87], a[36:39], v[52:55]\n"
    "buffer_load_dwordx4 %[voffv], %[srdv], s80 offen nt lds\n"
    "v_mfma_f32_16x16x32_bf16 v[52:55], a[88:91], a[40:43], v[52:55]\n"
    "v_exp_f32 v43, v43\n"
    "v_mfma_f32_16x16x32_bf16 v[52:55], a[92:95], a[44:47], v[52:55]\n"
    "ds_read_b64_tr_b16 a[144:145], v120 offset:32768\n"
    "v_cvt_pk_bf16_f32 v73, v42, v43\n"
    "v_mfma_f32_16x16x32_bf16 v[192:195], a[160:163], v[84:87], v[192:195]\n"
    "v_exp_f32 v44, v44\n"
    "v_mfma_f32_16x16x32_bf16 v[196:199], a[164:167], v[84:87], v[196:199]\n"
    "ds_read_b64_tr_b16 a[146:147], v120 offset:36864\n"
    "v_mfma_f32_16x16x32_bf16 v[200:203], a[168:171], v[84:87], v[200:203]\n"
    "v_exp_f32 v45, v45\n"
    "v_mfma_f32_16x16x32_bf16 v[204:207], a[172:175], v[84:87], v[204:207]\n"
    "ds_read_b64_tr_b16 a[148:149], v121 offset:32768\n"
    "v_cvt_pk_bf16_f32 v74, v44, v45\n"
    "v_mfma_f32_16x16x32_bf16 v[208:211], a[176:179], v[84:87], v[208:211]\n"
    "v_exp_f32 v46, v46\n"
    "v_mfma_f32_16x16x32_bf16 v[212:215], a[180:183], v[84:87], v[212:215]\n"
    "ds_read_b64_tr_b16 a[150:151], v121 offset:36864\n"
    "v_mfma_f32_16x16x32_bf16 v[216:219], a[184:187], v[84:87], v[216:219]\n"
    "v_exp_f32 v47, v47\n"
    "v_mfma_f32_16x16x32_bf16 v[220:223], a[188:191], v[84:87], v[220:223]\n"
    "ds_read_b64_tr_b16 a[152:153], v122 offset:32768\n"
    "v_cvt_pk_bf16_f32 v75, v46, v47\n"
    "v_smfmac_f32_16x16x64_bf16 a[200:203], a[208:211], v[80:87], v126\n"
    "v_mfma_f32_16x16x32_bf16 v[56:59], a[64:67], a[48:51], v[108:111]\n"
    "v_exp_f32 v48, v48\n"
    "v_mfma_f32_16x16x32_bf16 v[56:59], a[68:71], a[52:55], v[56:59]\n"
    "ds_read_b64_tr_b16 a[154:155], v122 offset:36864\n"
    "v_mfma_f32_16x16x32_bf16 v[56:59], a[72:75], a[56:59], v[56:59]\n"
    "v_exp_f32 v49, v49\n"
    "v_mfma_f32_16x16x32_bf16 v[56:59], a[76:79], a[60:63], v[56:59]\n"
    "ds_read_b64_tr_b16 a[156:157], v123 offset:32768\n"
    "v_cvt_pk_bf16_f32 v80, v48, v49\n"
    "v_mfma_f32_16x16x32_bf16 v[60:63], a[80:83], a[48:51], v[108:111]\n"
    "v_exp_f32 v50, v50\n"
    "v_mfma_f32_16x16x32_bf16 v[60:63], a[84:87], a[52:55], v[60:63]\n"
    "buffer_load_dwordx4 v125, %[srdv], s80 offen offset:1024 nt lds\n"
    "v_mfma_f32_16x16x32_bf16 v[60:63], a[88:91], a[56:59], v[60:63]\n"
    "v_exp_f32 v51, v51\n"
    "v_mfma_f32_16x16x32_bf16 v[60:63], a[92:95], a[60:63], v[60:63]\n"
    "ds_read_b64_tr_b16 a[158:159], v123 offset:36864\n"
    "v_cvt_pk_bf16_f32 v81, v50, v51\n"
    "v_mfma_f32_16x16x32_bf16 v[224:227], a[160:163], v[92:95], v[224:227]\n"
    "v_exp_f32 v52, v52\n"
    "v_mfma_f32_16x16x32_bf16 v[228:231], a[164:167], v[92:95], v[228:231]\n"
    "s_add_u32 s80, s80, 65536\n"
    "v_mfma_f32_16x16x32_bf16 v[232:235], a[168:171], v[92:95], v[232:235]\n"
    "v_exp_f32 v53, v53\n"
    "v_mfma_f32_16x16x32_bf16 v[236:239], a[172:175], v[92:95], v[236:239]\n"
    "v_cvt_pk_bf16_f32 v82, v52, v53\n"
    "v_mfma_f32_16x16x32_bf16 v[240:243], a[176:179], v[92:95], v[240:243]\n"
    "v_exp_f32 v54, v54\n"
    "v_mfma_f32_16x16x32_bf16 v[244:247], a[180:183], v[92:95], v[244:247]\n"
    "v_mfma_f32_16x16x32_bf16 v[248:251], a[184:187], v[92:95], v[248:251]\n"
    "v_exp_f32 v55, v55\n"
    "v_mfma_f32_16x16x32_bf16 v[252:255], a[188:191], v[92:95], v[252:255]\n"
    "v_cvt_pk_bf16_f32 v83, v54, v55\n"
    "v_smfmac_f32_16x16x64_bf16 a[204:207], a[208:211], v[88:95], v126\n"
    "s_branch Lloop%=\n"
    "Ltail%=:\n"
    "Lh7_%=:\n"
    "s_cbranch_execz Lr7_%=\n"
    "s_waitcnt lgkmcnt(0)\n"
    "; -- A24a\n"
    "v_mfma_f32_16x16x32_bf16 v[32:35], a[64:67], a[0:3], v[96:99]\n"
    "v_exp_f32 v56, v56\n"
    "v_mfma_f32_16x16x32_bf16 v[32:35], a[68:71], a[4:7], v[32:35]\n"
    "s_waitcnt vmcnt(12)\n"
    "s_barrier\n"
    "v_mfma_f32_16x16x32_bf16 v[32:35], a[72:75], a[8:11], v[32:35]\n"
    "v_exp_f32 v57, v57\n"
    "v_mfma_f32_16x16x32_bf16 v[32:35], a[76:79], a[12:15], v[32:35]\n"
    "s_mov_b32 s80, 1966080\n"
    "v_cvt_pk_bf16_f32 v92, v56, v57\n"
    "v_mfma_f32_16x16x32_bf16 v[36:39], a[80:83], a[0:3], v[96:99]\n"
    "v_exp_f32 v58, v58\n"
    "v_mfma_f32_16x16x32_bf16 v[36:39], a[84:87], a[4:7], v[36:39]\n"
    "s_add_u32 m0, %[m0w], 49152\n"
    "s_nop 0\n"
    "buffer_load_dwordx4 %[voffk], %[srdk], s80 offen nt lds\n"
    "v_mfma_f32_16x16x32_bf16 v[36:39], a[88:91], a[8:11], v[36:39]\n"
    "v_exp_f32 v59, v59\n"
    "v_mfma_f32_16x16x32_bf16 v[36:39], a[92:95], a[12:15], v[36:39]\n"
    "buffer_load_dwordx4 v124, %[srdk], s80 offen offset:1024 nt lds\n"
    "v_cvt_pk_bf16_f32 v93, v58, v59\n"
    "v_mfma_f32_16x16x32_bf16 v[128:131], a[160:163], v[68:71], v[128:131]\n"
    "v_exp_f32 v60, v60\n"
    "v_mfma_f32_16x16x32_bf16 v[132:135], a[164:167], v[68:71], v[132:135]\n"
    "ds_read_b128 a[96:99], v112 offset:8192\n"
    "ds_read_b128 a[100:103], v113 offset:8192\n"
    "v_mfma_f32_16x16x32_bf16 v[136:139], a[168:171], v[68:71], v[136:139]\n"
    "v_exp_f32 v61, v61\n"
    "v_mfma_f32_16x16x32_bf16 v[140:143], a[172:175], v[68:71], v[140:143]\n"
    "ds_read_b128 a[104:107], v114 offset:8192\n"
    "v_cvt_pk_bf16_f32 v94, v60, v61\n"
    "v_mfma_f32_16x16x32_bf16 v[144:147], a[176:179], v[68:71], v[144:147]\n"
    "v_exp_f32 v62, v62\n"
    "v_mfma_f32_16x16x32_bf16 v[148:151], a[180:183], v[68:71], v[148:151]\n"
    "ds_read_b128 a[108:111], v115 offset:8192\n"
    "ds_read_b128 a[112:115], v112 offset:12288\n"
    "v_mfma_f32_16x16x32_bf16 v[152:155], a[184:187], v[68:71], v[152:155]\n"
    "v_exp_f32 v63, v63\n"
    "v_mfma_f32_16x16x32_bf16 v[156:159], a[188:191], v[68:71], v[156:159]\n"
    "ds_read_b128 a[116:119], v113 offset:12288\n"
    "v_cvt_pk_bf16_f32 v95, v62, v63\n"
    "v_mfma_f32_16x16x32_bf16 v[160:163], a[160:163], v[76:79], v[160:163]\n"
    "ds_read_b128 a[120:123], v114 offset:12288\n"
    "ds_read_b128 a[124:127], v115 offset:12288\n"
    "v_mfma_f32_16x16x32_bf16 v[164:167], a[164:167], v[76:79], v[164:167]\n"
    "ds_read_b64_tr_b16 a[128:129], v116 offset:0\n"
    "ds_read_b64_tr_b16 a[130:131], v116 offset:4096\n"
    "v_mfma_f32_16x16x32_bf16 v[168:171], a[168:171], v[76:79], v[168:171]\n"
    "ds_read_b64_tr_b16 a[132:133], v117 offset:0\n"
    "v_mfma_f32_16x16x32_bf16 v[172:175], a[172:175], v[76:79], v[172:175]\n"
    "v_mfma_f32_16x16x32_bf16 v[176:179], a[176:179], v[76:79], v[176:179]\n"
    "v_mfma_f32_16x16x32_bf16 v[180:183], a[180:183], v[76:79], v[180:183]\n"
    "v_mfma_f32_16x16x32_bf16 v[184:187], a[184:187], v[76:79], v[184:187]\n"
    "v_mfma_f32_16x16x32_bf16 v[188:191], a[188:191], v[76:79], v[188:191]\n"
    "v_smfmac_f32_16x16x64_bf16 a[192:195], a[208:211], v[64:71], v126\n"
    "v_smfmac_f32_16x16x64_bf16 a[196:199], a[208:211], v[72:79], v126\n"
    "; -- A24b\n"
    "v_mfma_f32_16x16x32_bf16 v[48:51], a[64:67], a[32:35], v[104:107]\n"
    "v_exp_f32 v32, v32\n"
    "v_mfma_f32_16x16x32_bf16 v[48:51], a[68:71], a[36:39], v[48:51]\n"
    "s_add_u32 m0, %[m0w], 106496\n"
    "s_nop 0\n"
    "buffer_load_dwordx4 %[voffv], %[srdv], s80 offen nt lds\n"
    "v_mfma_f32_16x16x32_bf16 v[48:51], a[72:75], a[40:43], v[48:51]\n"
    "v_exp_f32 v33, v33\n"
    "v_mfma_f32_16x16x32_bf16 v[48:51], a[76:79], a[44:47], v[48:51]\n"
    "buffer_load_dwordx4 v125, %[srdv], s80 offen offset:1024 nt lds\n"
    "v_cvt_pk_bf16_f32 v64, v32, v33\n"
    "v_mfma_f32_16x16x32_bf16 v[52:55], a[80:83], a[32:35], v[104:107]\n"
    "v_exp_f32 v34, v34\n"
    "v_mfma_f32_16x16x32_bf16 v[52:55], a[84:87], a[36:39], v[52:55]\n"
    "ds_read_b64_tr_b16 a[134:135], v117 offset:4096\n"
    "ds_read_b64_tr_b16 a[136:137], v118 offset:0\n"
    "v_mfma_f32_16x16x32_bf16 v[52:55], a[88:91], a[40:43], v[52:55]\n"
    "v_exp_f32 v35, v35\n"
    "v_mfma_f32_16x16x32_bf16 v[52:55], a[92:95], a[44:47], v[52:55]\n"
    "ds_read_b64_tr_b16 a[138:139], v118 offset:4096\n"
    "v_cvt_pk_bf16_f32 v65, v34, v35\n"
    "v_mfma_f32_16x16x32_bf16 v[192:195], a[160:163], v[84:87], v[192:195]\n"
    "v_exp_f32 v36, v36\n"
    "v_mfma_f32_16x16x32_bf16 v[196:199], a[164:167], v[84:87], v[196:199]\n"
    "ds_read_b64_tr_b16 a[140:141], v119 offset:0\n"
    "ds_read_b64_tr_b16 a[142:143], v119 offset:4096\n"
    "v_mfma_f32_16x16x32_bf16 v[200:203], a[168:171], v[84:87], v[200:203]\n"
    "v_exp_f32 v37, v37\n"
    "v_mfma_f32_16x16x32_bf16 v[204:207], a[172:175], v[84:87], v[204:207]\n"
    "ds_read_b64_tr_b16 a[144:145], v120 offset:0\n"
    "v_cvt_pk_bf16_f32 v66, v36, v37\n"
    "v_mfma_f32_16x16x32_bf16 v[208:211], a[176:179], v[84:87], v[208:211]\n"
    "v_exp_f32 v38, v38\n"
    "v_mfma_f32_16x16x32_bf16 v[212:215], a[180:183], v[84:87], v[212:215]\n"
    "ds_read_b64_tr_b16 a[146:147], v120 offset:4096\n"
    "ds_read_b64_tr_b16 a[148:149], v121 offset:0\n"
    "v_mfma_f32_16x16x32_bf16 v[216:219], a[184:187], v[84:87], v[216:219]\n"
    "v_exp_f32 v39, v39\n"
    "v_mfma_f32_16x16x32_bf16 v[220:223], a[188:191], v[84:87], v[220:223]\n"
    "ds_read_b64_tr_b16 a[150:151], v121 offset:4096\n"
    "v_cvt_pk_bf16_f32 v67, v38, v39\n"
    "v_mfma_f32_16x16x32_bf16 v[224:227], a[160:163], v[92:95], v[224:227]\n"
    "ds_read_b64_tr_b16 a[152:153], v122 offset:0\n"
    "ds_read_b64_tr_b16 a[154:155], v122 offset:4096\n"
    "v_mfma_f32_16x16x32_bf16 v[228:231], a[164:167], v[92:95], v[228:231]\n"
    "ds_read_b64_tr_b16 a[156:157], v123 offset:0\n"
    "ds_read_b64_tr_b16 a[158:159], v123 offset:4096\n"
    "v_mfma_f32_16x16x32_bf16 v[232:235], a[168:171], v[92:95], v[232:235]\n"
    "v_mfma_f32_16x16x32_bf16 v[236:239], a[172:175], v[92:95], v[236:239]\n"
    "v_mfma_f32_16x16x32_bf16 v[240:243], a[176:179], v[92:95], v[240:243]\n"
    "v_mfma_f32_16x16x32_bf16 v[244:247], a[180:183], v[92:95], v[244:247]\n"
    "v_mfma_f32_16x16x32_bf16 v[248:251], a[184:187], v[92:95], v[248:251]\n"
    "v_mfma_f32_16x16x32_bf16 v[252:255], a[188:191], v[92:95], v[252:255]\n"
    "v_smfmac_f32_16x16x64_bf16 a[200:203], a[208:211], v[80:87], v126\n"
    "v_smfmac_f32_16x16x64_bf16 a[204:207], a[208:211], v[88:95], v126\n"
    "; -- A25a\n"
    "s_waitcnt lgkmcnt(7)\n"
    "v_mfma_f32_16x16x32_bf16 v[32:35], a[96:99], a[0:3], v[96:99]\n"
    "v_exp_f32 v48, v48\n"
    "v_mfma_f32_16x16x32_bf16 v[32:35], a[100:103], a[4:7], v[32:35]\n"
    "s_mov_b32 s80, 2031616\n"
    "s_add_u32 m0, %[m0w], 57344\n"
    "v_mfma_f32_16x16x32_bf16 v[32:35], a[104:107], a[8:11], v[32:35]\n"
    "v_exp_f32 v49, v49\n"
    "v_mfma_f32_16x16x32_bf16 v[32:35], a[108:111], a[12:15], v[32:35]\n"
    "buffer_load_dwordx4 %[voffk], %[srdk], s80 offen nt lds\n"
    "v_cvt_pk_bf16_f32 v80, v48, v49\n"
    "v_mfma_f32_16x16x32_bf16 v[36:39], a[112:115], a[0:3], v[96:99]\n"
    "v_exp_f32 v50, v50\n"
    "v_mfma_f32_16x16x32_bf16 v[36:39], a[116:119], a[4:7], v[36:39]\n"
    "buffer_load_dwordx4 v124, %[srdk], s80 offen offset:1024 nt lds\n"
    "v_mfma_f32_16x16x32_bf16 v[36:39], a[120:123], a[8:11], v[36:39]\n"
    "v_exp_f32 v51, v51\n"
    "v_mfma_f32_16x16x32_bf16 v[36:39], a[124:127], a[12:15], v[36:39]\n"
    "ds_read_b128 a[64:67], v112 offset:16384\n"
    "v_cvt_pk_bf16_f32 v81, v50, v51\n"
    "v_mfma_f32_16x16x32_bf16 v[128:131], a[128:131], v[64:67], v[128:131]\n"
    "v_exp_f32 v52, v52\n"
    "v_mfma_f32_16x16x32_bf16 v[132:135], a[132:135], v[64:67], v[132:135]\n"
    "ds_read_b128 a[68:71], v113 offset:16384\n"
    "ds_read_b128 a[72:75], v114 offset:16384\n"
    "v_mfma_f32_16x16x32_bf16 v[136:139], a[136:139], v[64:67], v[136:139]\n"
    "v_exp_f32 v53, v53\n"
    "v_mfma_f32_16x16x32_bf16 v[140:143], a[140:143], v[64:67], v[140:143]\n"
    "ds_read_b128 a[76:79], v115 offset:16384\n"
    "v_cvt_pk_bf16_f32 v82, v52, v53\n"
    "s_waitcnt lgkmcnt(4)\n"
    "v_mfma_f32_16x16x32_bf16 v[144:147], a[144:147], v[64:67], v[144:147]\n"
    "v_exp_f32 v54, v54\n"
    "v_mfma_f32_16x16x32_bf16 v[148:151], a[148:151], v[64:67], v[148:151]\n"
    "ds_read_b128 a[80:83], v112 offset:20480\n"
    "ds_read_b128 a[84:87], v113 offset:20480\n"
    "v_mfma_f32_16x16x32_bf16 v[152:155], a[152:155], v[64:67], v[152:155]\n"
    "v_exp_f32 v55, v55\n"
    "v_mfma_f32_16x16x32_bf16 v[156:159], a[156:159], v[64:67], v[156:159]\n"
    "ds_read_b128 a[88:91], v114 offset:20480\n"
    "v_cvt_pk_bf16_f32 v83, v54, v55\n"
    "ds_read_b128 a[92:95], v115 offset:20480\n"
    "ds_read_b64_tr_b16 a[160:161], v116 offset:8192\n"
    "ds_read_b64_tr_b16 a[162:163], v116 offset:12288\n"
    "ds_read_b64_tr_b16 a[164:165], v117 offset:8192\n"
    "ds_read_b64_tr_b16 a[166:167], v117 offset:12288\n"
    "; -- A25b\n"
    "v_mfma_f32_16x16x32_bf16 v[48:51], a[96:99], a[32:35], v[104:107]\n"
    "v_exp_f32 v32, v32\n"
    "v_mfma_f32_16x16x32_bf16 v[48:51], a[100:103], a[36:39], v[48:51]\n"
    "s_add_u32 m0, %[m0w], 114688\n"
    "s_nop 0\n"
    "buffer_load_dwordx4 %[voffv], %[srdv], s80 offen nt lds\n"
    "v_mfma_f32_16x16x32_bf16 v[48:51], a[104:107], a[40:43], v[48:51]\n"
    "v_exp_f32 v33, v33\n"
    "v_mfma_f32_16x16x32_bf16 v[48:51], a[108:111], a[44:47], v[48:51]\n"
    "buffer_load_dwordx4 v125, %[srdv], s80 offen offset:1024 nt lds\n"
    "v_cvt_pk_bf16_f32 v68, v32, v33\n"
    "v_mfma_f32_16x16x32_bf16 v[52:55], a[112:115], a[32:35], v[104:107]\n"
    "v_exp_f32 v34, v34\n"
    "v_mfma_f32_16x16x32_bf16 v[52:55], a[116:119], a[36:39], v[52:55]\n"
    "ds_read_b64_tr_b16 a[168:169], v118 offset:8192\n"
    "ds_read_b64_tr_b16 a[170:171], v118 offset:12288\n"
    "v_mfma_f32_16x16x32_bf16 v[52:55], a[120:123], a[40:43], v[52:55]\n"
    "v_exp_f32 v35, v35\n"
    "v_mfma_f32_16x16x32_bf16 v[52:55], a[124:127], a[44:47], v[52:55]\n"
    "ds_read_b64_tr_b16 a[172:173], v119 offset:8192\n"
    "v_cvt_pk_bf16_f32 v69, v34, v35\n"
    "v_mfma_f32_16x16x32_bf16 v[192:195], a[128:131], v[80:83], v[192:195]\n"
    "v_exp_f32 v36, v36\n"
    "v_mfma_f32_16x16x32_bf16 v[196:199], a[132:135], v[80:83], v[196:199]\n"
    "ds_read_b64_tr_b16 a[174:175], v119 offset:12288\n"
    "ds_read_b64_tr_b16 a[176:177], v120 offset:8192\n"
    "v_mfma_f32_16x16x32_bf16 v[200:203], a[136:139], v[80:83], v[200:203]\n"
    "v_exp_f32 v37, v37\n"
    "v_mfma_f32_16x16x32_bf16 v[204:207], a[140:143], v[80:83], v[204:207]\n"
    "ds_read_b64_tr_b16 a[178:179], v120 offset:12288\n"
    "v_cvt_pk_bf16_f32 v70, v36, v37\n"
    "v_mfma_f32_16x16x32_bf16 v[208:211], a[144:147], v[80:83], v[208:211]\n"
    "v_exp_f32 v38, v38\n"
    "v_mfma_f32_16x16x32_bf16 v[212:215], a[148:151], v[80:83], v[212:215]\n"
    "ds_read_b64_tr_b16 a[180:181], v121 offset:8192\n"
    "ds_read_b64_tr_b16 a[182:183], v121 offset:12288\n"
    "v_mfma_f32_16x16x32_bf16 v[216:219], a[152:155], v[80:83], v[216:219]\n"
    "v_exp_f32 v39, v39\n"
    "v_mfma_f32_16x16x32_bf16 v[220:223], a[156:159], v[80:83], v[220:223]\n"
    "ds_read_b64_tr_b16 a[184:185], v122 offset:8192\n"
    "v_cvt_pk_bf16_f32 v71, v38, v39\n"
    "ds_read_b64_tr_b16 a[186:187], v122 offset:12288\n"
    "ds_read_b64_tr_b16 a[188:189], v123 offset:8192\n"
    "ds_read_b64_tr_b16 a[190:191], v123 offset:12288\n"
    "; -- A26a\n"
    "s_waitcnt lgkmcnt(9)\n"
    "v_mfma_f32_16x16x32_bf16 v[32:35], a[64:67], a[0:3], v[96:99]\n"
    "v_exp_f32 v48, v48\n"
    "v_mfma_f32_16x16x32_bf16 v[32:35], a[68:71], a[4:7], v[32:35]\n"
    "s_waitcnt vmcnt(12)\n"
    "s_barrier\n"
    "v_mfma_f32_16x16x32_bf16 v[32:35], a[72:75], a[8:11], v[32:35]\n"
    "v_exp_f32 v49, v49\n"
    "v_mfma_f32_16x16x32_bf16 v[32:35], a[76:79], a[12:15], v[32:35]\n"
    "s_mov_b32 s80, 2097152\n"
    "v_cvt_pk_bf16_f32 v84, v48, v49\n"
    "v_mfma_f32_16x16x32_bf16 v[36:39], a[80:83], a[0:3], v[96:99]\n"
    "v_exp_f32 v50, v50\n"
    "v_mfma_f32_16x16x32_bf16 v[36:39], a[84:87], a[4:7], v[36:39]\n"
    "s_add_u32 m0, %[m0w], 122880\n"
    "s_nop 0\n"
    "buffer_load_dwordx4 %[voffv], %[srdv], s80 offen nt lds\n"
    "v_mfma_f32_16x16x32_bf16 v[36:39], a[88:91], a[8:11], v[36:39]\n"
    "v_exp_f32 v51, v51\n"
    "v_mfma_f32_16x16x32_bf16 v[36:39], a[92:95], a[12:15], v[36:39]\n"
    "buffer_load_dwordx4 v125, %[srdv], s80 offen offset:1024 nt lds\n"
    "v_cvt_pk_bf16_f32 v85, v50, v51\n"
    "v_mfma_f32_16x16x32_bf16 v[128:131], a[160:163], v[68:71], v[128:131]\n"
    "v_exp_f32 v52, v52\n"
    "v_mfma_f32_16x16x32_bf16 v[132:135], a[164:167], v[68:71], v[132:135]\n"
    "ds_read_b128 a[96:99], v112 offset:24576\n"
    "ds_read_b128 a[100:103], v113 offset:24576\n"
    "v_mfma_f32_16x16x32_bf16 v[136:139], a[168:171], v[68:71], v[136:139]\n"
    "v_exp_f32 v53, v53\n"
    "s_waitcnt lgkmcnt(2)\n"
    "v_mfma_f32_16x16x32_bf16 v[140:143], a[172:175], v[68:71], v[140:143]\n"
    "ds_read_b128 a[104:107], v114 offset:24576\n"
    "v_cvt_pk_bf16_f32 v86, v52, v53\n"
    "v_mfma_f32_16x16x32_bf16 v[144:147], a[176:179], v[68:71], v[144:147]\n"
    "v_exp_f32 v54, v54\n"
    "v_mfma_f32_16x16x32_bf16 v[148:151], a[180:183], v[68:71], v[148:151]\n"
    "ds_read_b128 a[108:111], v115 offset:24576\n"
    "ds_read_b128 a[112:115], v112 offset:28672\n"
    "v_mfma_f32_16x16x32_bf16 v[152:155], a[184:187], v[68:71], v[152:155]\n"
    "v_exp_f32 v55, v55\n"
    "v_mfma_f32_16x16x32_bf16 v[156:159], a[188:191], v[68:71], v[156:159]\n"
    "ds_read_b128 a[116:119], v113 offset:28672\n"
    "v_cvt_pk_bf16_f32 v87, v54, v55\n"
    "v_smfmac_f32_16x16x64_bf16 a[192:195], a[208:211], v[64:71], v126\n"
    "ds_read_b128 a[120:123], v114 offset:28672\n"
    "ds_read_b128 a[124:127], v115 offset:28672\n"
    "ds_read_b64_tr_b16 a[128:129], v116 offset:16384\n"
    "ds_read_b64_tr_b16 a[130:131], v116 offset:20480\n"
    "ds_read_b64_tr_b16 a[132:133], v117 offset:16384\n"
    "; -- A26b\n"
    "v_mfma_f32_16x16x32_bf16 v[48:51], a[64:67], a[32:35], v[104:107]\n"
    "v_exp_f32 v32, v32\n"
    "v_mfma_f32_16x16x32_bf16 v[48:51], a[68:71], a[36:39], v[48:51]\n"
    "ds_read_b64_tr_b16 a[134:135], v117 offset:20480\n"
    "ds_read_b64_tr_b16 a[136:137], v118 offset:16384\n"
    "v_mfma_f32_16x16x32_bf16 v[48:51], a[72:75], a[40:43], v[48:51]\n"
    "v_exp_f32 v33, v33\n"
    "v_mfma_f32_16x16x32_bf16 v[48:51], a[76:79], a[44:47], v[48:51]\n"
    "ds_read_b64_tr_b16 a[138:139], v118 offset:20480\n"
    "v_cvt_pk_bf16_f32 v64, v32, v33\n"
    "v_mfma_f32_16x16x32_bf16 v[52:55], a[80:83], a[32:35], v[104:107]\n"
    "v_exp_f32 v34, v34\n"
    "v_mfma_f32_16x16x32_bf16 v[52:55], a[84:87], a[36:39], v[52:55]\n"
    "ds_read_b64_tr_b16 a[140:141], v119 offset:16384\n"
    "ds_read_b64_tr_b16 a[142:143], v119 offset:20480\n"
    "v_mfma_f32_16x16x32_bf16 v[52:55], a[88:91], a[40:43], v[52:55]\n"
    "v_exp_f32 v35, v35\n"
    "v_mfma_f32_16x16x32_bf16 v[52:55], a[92:95], a[44:47], v[52:55]\n"
    "ds_read_b64_tr_b16 a[144:145], v120 offset:16384\n"
    "v_cvt_pk_bf16_f32 v65, v34, v35\n"
    "v_mfma_f32_16x16x32_bf16 v[192:195], a[160:163], v[84:87], v[192:195]\n"
    "v_exp_f32 v36, v36\n"
    "v_mfma_f32_16x16x32_bf16 v[196:199], a[164:167], v[84:87], v[196:199]\n"
    "ds_read_b64_tr_b16 a[146:147], v120 offset:20480\n"
    "ds_read_b64_tr_b16 a[148:149], v121 offset:16384\n"
    "v_mfma_f32_16x16x32_bf16 v[200:203], a[168:171], v[84:87], v[200:203]\n"
    "v_exp_f32 v37, v37\n"
    "v_mfma_f32_16x16x32_bf16 v[204:207], a[172:175], v[84:87], v[204:207]\n"
    "ds_read_b64_tr_b16 a[150:151], v121 offset:20480\n"
    "v_cvt_pk_bf16_f32 v66, v36, v37\n"
    "v_mfma_f32_16x16x32_bf16 v[208:211], a[176:179], v[84:87], v[208:211]\n"
    "v_exp_f32 v38, v38\n"
    "v_mfma_f32_16x16x32_bf16 v[212:215], a[180:183], v[84:87], v[212:215]\n"
    "ds_read_b64_tr_b16 a[152:153], v122 offset:16384\n"
    "ds_read_b64_tr_b16 a[154:155], v122 offset:20480\n"
    "v_mfma_f32_16x16x32_bf16 v[216:219], a[184:187], v[84:87], v[216:219]\n"
    "v_exp_f32 v39, v39\n"
    "v_mfma_f32_16x16x32_bf16 v[220:223], a[188:191], v[84:87], v[220:223]\n"
    "ds_read_b64_tr_b16 a[156:157], v123 offset:16384\n"
    "v_cvt_pk_bf16_f32 v67, v38, v39\n"
    "v_smfmac_f32_16x16x64_bf16 a[200:203], a[208:211], v[80:87], v126\n"
    "ds_read_b64_tr_b16 a[158:159], v123 offset:20480\n"
    "; -- A27a\n"
    "s_waitcnt lgkmcnt(7)\n"
    "v_mfma_f32_16x16x32_bf16 v[32:35], a[96:99], a[0:3], v[96:99]\n"
    "v_exp_f32 v48, v48\n"
    "v_mfma_f32_16x16x32_bf16 v[32:35], a[100:103], a[4:7], v[32:35]\n"
    "ds_read_b128 a[64:67], v112 offset:32768\n"
    "ds_read_b128 a[68:71], v113 offset:32768\n"
    "v_mfma_f32_16x16x32_bf16 v[32:35], a[104:107], a[8:11], v[32:35]\n"
    "v_exp_f32 v49, v49\n"
    "v_mfma_f32_16x16x32_bf16 v[32:35], a[108:111], a[12:15], v[32:35]\n"
    "ds_read_b128 a[72:75], v114 offset:32768\n"
    "v_cvt_pk_bf16_f32 v80, v48, v49\n"
    "v_mfma_f32_16x16x32_bf16 v[36:39], a[112:115], a[0:3], v[96:99]\n"
    "v_exp_f32 v50, v50\n"
    "v_mfma_f32_16x16x32_bf16 v[36:39], a[116:119], a[4:7], v[36:39]\n"
    "ds_read_b128 a[76:79], v115 offset:32768\n"
    "ds_read_b128 a[80:83], v112 offset:36864\n"
    "v_mfma_f32_16x16x32_bf16 v[36:39], a[120:123], a[8:11], v[36:39]\n"
    "v_exp_f32 v51, v51\n"
    "v_mfma_f32_16x16x32_bf16 v[36:39], a[124:127], a[12:15], v[36:39]\n"
    "ds_read_b128 a[84:87], v113 offset:36864\n"
    "v_cvt_pk_bf16_f32 v81, v50, v51\n"
    "v_mfma_f32_16x16x32_bf16 v[128:131], a[128:131], v[64:67], v[128:131]\n"
    "v_exp_f32 v52, v52\n"
    "v_mfma_f32_16x16x32_bf16 v[132:135], a[132:135], v[64:67], v[132:135]\n"
    "ds_read_b128 a[88:91], v114 offset:36864\n"
    "ds_read_b128 a[92:95], v115 offset:36864\n"
    "v_mfma_f32_16x16x32_bf16 v[136:139], a[136:139], v[64:67], v[136:139]\n"
    "v_exp_f32 v53, v53\n"
    "v_mfma_f32_16x16x32_bf16 v[140:143], a[140:143], v[64:67], v[140:143]\n"
    "ds_read_b64_tr_b16 a[160:161], v116 offset:24576\n"
    "v_cvt_pk_bf16_f32 v82, v52, v53\n"
    "s_waitcnt lgkmcnt(7)\n"
    "v_mfma_f32_16x16x32_bf16 v[144:147], a[144:147], v[64:67], v[144:147]\n"
    "v_exp_f32 v54, v54\n"
    "v_mfma_f32_16x16x32_bf16 v[148:151], a[148:151], v[64:67], v[148:151]\n"
    "ds_read_b64_tr_b16 a[162:163], v116 offset:28672\n"
    "ds_read_b64_tr_b16 a[164:165], v117 offset:24576\n"
    "v_mfma_f32_16x16x32_bf16 v[152:155], a[152:155], v[64:67], v[152:155]\n"
    "v_exp_f32 v55, v55\n"
    "v_mfma_f32_16x16x32_bf16 v[156:159], a[156:159], v[64:67], v[156:159]\n"
    "ds_read_b64_tr_b16 a[166:167], v117 offset:28672\n"
    "v_cvt_pk_bf16_f32 v83, v54, v55\n"
    "; -- A27b\n"
    "v_mfma_f32_16x16x32_bf16 v[48:51], a[96:99], a[32:35], v[104:107]\n"
    "v_exp_f32 v32, v32\n"
    "v_mfma_f32_16x16x32_bf16 v[48:51], a[100:103], a[36:39], v[48:51]\n"
    "ds_read_b64_tr_b16 a[168:169], v118 offset:24576\n"
    "ds_read_b64_tr_b16 a[170:171], v118 offset:28672\n"
    "v_mfma_f32_16x16x32_bf16 v[48:51], a[104:107], a[40:43], v[48:51]\n"
    "v_exp_f32 v33, v33\n"
    "v_mfma_f32_16x16x32_bf16 v[48:51], a[108:111], a[44:47], v[48:51]\n"
    "ds_read_b64_tr_b16 a[172:173], v119 offset:24576\n"
    "v_cvt_pk_bf16_f32 v68, v32, v33\n"
    "v_mfma_f32_16x16x32_bf16 v[52:55], a[112:115], a[32:35], v[104:107]\n"
    "v_exp_f32 v34, v34\n"
    "v_mfma_f32_16x16x32_bf16 v[52:55], a[116:119], a[36:39], v[52:55]\n"
    "ds_read_b64_tr_b16 a[174:175], v119 offset:28672\n"
    "ds_read_b64_tr_b16 a[176:177], v120 offset:24576\n"
    "v_mfma_f32_16x16x32_bf16 v[52:55], a[120:123], a[40:43], v[52:55]\n"
    "v_exp_f32 v35, v35\n"
    "v_mfma_f32_16x16x32_bf16 v[52:55], a[124:127], a[44:47], v[52:55]\n"
    "ds_read_b64_tr_b16 a[178:179], v120 offset:28672\n"
    "v_cvt_pk_bf16_f32 v69, v34, v35\n"
    "v_mfma_f32_16x16x32_bf16 v[192:195], a[128:131], v[80:83], v[192:195]\n"
    "v_exp_f32 v36, v36\n"
    "v_mfma_f32_16x16x32_bf16 v[196:199], a[132:135], v[80:83], v[196:199]\n"
    "ds_read_b64_tr_b16 a[180:181], v121 offset:24576\n"
    "ds_read_b64_tr_b16 a[182:183], v121 offset:28672\n"
    "v_mfma_f32_16x16x32_bf16 v[200:203], a[136:139], v[80:83], v[200:203]\n"
    "v_exp_f32 v37, v37\n"
    "v_mfma_f32_16x16x32_bf16 v[204:207], a[140:143], v[80:83], v[204:207]\n"
    "ds_read_b64_tr_b16 a[184:185], v122 offset:24576\n"
    "v_cvt_pk_bf16_f32 v70, v36, v37\n"
    "v_mfma_f32_16x16x32_bf16 v[208:211], a[144:147], v[80:83], v[208:211]\n"
    "v_exp_f32 v38, v38\n"
    "v_mfma_f32_16x16x32_bf16 v[212:215], a[148:151], v[80:83], v[212:215]\n"
    "ds_read_b64_tr_b16 a[186:187], v122 offset:28672\n"
    "ds_read_b64_tr_b16 a[188:189], v123 offset:24576\n"
    "v_mfma_f32_16x16x32_bf16 v[216:219], a[152:155], v[80:83], v[216:219]\n"
    "v_exp_f32 v39, v39\n"
    "v_mfma_f32_16x16x32_bf16 v[220:223], a[156:159], v[80:83], v[220:223]\n"
    "ds_read_b64_tr_b16 a[190:191], v123 offset:28672\n"
    "v_cvt_pk_bf16_f32 v71, v38, v39\n"
    "; -- A28a\n"
    "v_mfma_f32_16x16x32_bf16 v[32:35], a[64:67], a[0:3], v[96:99]\n"
    "v_exp_f32 v48, v48\n"
    "v_mfma_f32_16x16x32_bf16 v[32:35], a[68:71], a[4:7], v[32:35]\n"
    "s_waitcnt vmcnt(6)\n"
    "s_barrier\n"
    "s_waitcnt lgkmcnt(6)\n"
    "v_mfma_f32_16x16x32_bf16 v[32:35], a[72:75], a[8:11], v[32:35]\n"
    "v_exp_f32 v49, v49\n"
    "v_mfma_f32_16x16x32_bf16 v[32:35], a[76:79], a[12:15], v[32:35]\n"
    "ds_read_b128 a[96:99], v112 offset:40960\n"
    "v_cvt_pk_bf16_f32 v84, v48, v49\n"
    "v_mfma_f32_16x16x32_bf16 v[36:39], a[80:83], a[0:3], v[96:99]\n"
    "v_exp_f32 v50, v50\n"
    "v_mfma_f32_16x16x32_bf16 v[36:39], a[84:87], a[4:7], v[36:39]\n"
    "ds_read_b128 a[100:103], v113 offset:40960\n"
    "ds_read_b128 a[104:107], v114 offset:40960\n"
    "v_mfma_f32_16x16x32_bf16 v[36:39], a[88:91], a[8:11], v[36:39]\n"
    "v_exp_f32 v51, v51\n"
    "v_mfma_f32_16x16x32_bf16 v[36:39], a[92:95], a[12:15], v[36:39]\n"
    "ds_read_b128 a[108:111], v115 offset:40960\n"
    "v_cvt_pk_bf16_f32 v85, v50, v51\n"
    "v_mfma_f32_16x16x32_bf16 v[128:131], a[160:163], v[68:71], v[128:131]\n"
    "v_exp_f32 v52, v52\n"
    "v_mfma_f32_16x16x32_bf16 v[132:135], a[164:167], v[68:71], v[132:135]\n"
    "ds_read_b128 a[112:115], v112 offset:45056\n"
    "ds_read_b128 a[116:119], v113 offset:45056\n"
    "v_mfma_f32_16x16x32_bf16 v[136:139], a[168:171], v[68:71], v[136:139]\n"
    "v_exp_f32 v53, v53\n"
    "v_mfma_f32_16x16x32_bf16 v[140:143], a[172:175], v[68:71], v[140:143]\n"
    "ds_read_b128 a[120:123], v114 offset:45056\n"
    "v_cvt_pk_bf16_f32 v86, v52, v53\n"
    "v_mfma_f32_16x16x32_bf16 v[144:147], a[176:179], v[68:71], v[144:147]\n"
    "v_exp_f32 v54, v54\n"
    "s_waitcnt lgkmcnt(7)\n"
    "v_mfma_f32_16x16x32_bf16 v[148:151], a[180:183], v[68:71], v[148:151]\n"
    "ds_read_b128 a[124:127], v115 offset:45056\n"
    "ds_read_b64_tr_b16 a[128:129], v116 offset:32768\n"
    "v_mfma_f32_16x16x32_bf16 v[152:155], a[184:187], v[68:71], v[152:155]\n"
    "v_exp_f32 v55, v55\n"
    "v_mfma_f32_16x16x32_bf16 v[156:159], a[188:191], v[68:71], v[156:159]\n"
    "ds_read_b64_tr_b16 a[130:131], v116 offset:36864\n"
    "v_cvt_pk_bf16_f32 v87, v54, v55\n"
    "Lh8_%=:\n"
    "s_cbranch_execz Lr8_%=\n"
    "v_smfmac_f32_16x16x64_bf16 a[192:195], a[208:211], v[64:71], v126\n"
    "ds_read_b64_tr_b16 a[132:133], v117 offset:32768\n"
    "; -- A28b\n"
    "v_mfma_f32_16x16x32_bf16 v[48:51], a[64:67], a[32:35], v[104:107]\n"
    "v_exp_f32 v32, v32\n"
    "v_mfma_f32_16x16x32_bf16 v[48:51], a[68:71], a[36:39], v[48:51]\n"
    "ds_read_b64_tr_b16 a[134:135], v117 offset:36864\n"
    "ds_read_b64_tr_b16 a[136:137], v118 offset:32768\n"
    "v_mfma_f32_16x16x32_bf16 v[48:51], a[72:75], a[40:43], v[48:51]\n"
    "v_exp_f32 v33, v33\n"
    "v_mfma_f32_16x16x32_bf16 v[48:51], a[76:79], a[44:47], v[48:51]\n"
    "ds_read_b64_tr_b16 a[138:139], v118 offset:36864\n"
    "v_cvt_pk_bf16_f32 v64, v32, v33\n"
    "v_mfma_f32_16x16x32_bf16 v[52:55], a[80:83], a[32:35], v[104:107]\n"
    "v_exp_f32 v34, v34\n"
    "v_mfma_f32_16x16x32_bf16 v[52:55], a[84:87], a[36:39], v[52:55]\n"
    "ds_read_b64_tr_b16 a[140:141], v119 offset:32768\n"
    "ds_read_b64_tr_b16 a[142:143], v119 offset:36864\n"
    "v_mfma_f32_16x16x32_bf16 v[52:55], a[88:91], a[40:43], v[52:55]\n"
    "v_exp_f32 v35, v35\n"
    "v_mfma_f32_16x16x32_bf16 v[52:55], a[92:95], a[44:47], v[52:55]\n"
    "ds_read_b64_tr_b16 a[144:145], v120 offset:32768\n"
    "v_cvt_pk_bf16_f32 v65, v34, v35\n"
    "v_mfma_f32_16x16x32_bf16 v[192:195], a[160:163], v[84:87], v[192:195]\n"
    "v_exp_f32 v36, v36\n"
    "v_mfma_f32_16x16x32_bf16 v[196:199], a[164:167], v[84:87], v[196:199]\n"
    "ds_read_b64_tr_b16 a[146:147], v120 offset:36864\n"
    "ds_read_b64_tr_b16 a[148:149], v121 offset:32768\n"
    "v_mfma_f32_16x16x32_bf16 v[200:203], a[168:171], v[84:87], v[200:203]\n"
    "v_exp_f32 v37, v37\n"
    "v_mfma_f32_16x16x32_bf16 v[204:207], a[172:175], v[84:87], v[204:207]\n"
    "ds_read_b64_tr_b16 a[150:151], v121 offset:36864\n"
    "v_cvt_pk_bf16_f32 v66, v36, v37\n"
    "v_mfma_f32_16x16x32_bf16 v[208:211], a[176:179], v[84:87], v[208:211]\n"
    "v_exp_f32 v38, v38\n"
    "v_mfma_f32_16x16x32_bf16 v[212:215], a[180:183], v[84:87], v[212:215]\n"
    "ds_read_b64_tr_b16 a[152:153], v122 offset:32768\n"
    "ds_read_b64_tr_b16 a[154:155], v122 offset:36864\n"
    "v_mfma_f32_16x16x32_bf16 v[216:219], a[184:187], v[84:87], v[216:219]\n"
    "v_exp_f32 v39, v39\n"
    "v_mfma_f32_16x16x32_bf16 v[220:223], a[188:191], v[84:87], v[220:223]\n"
    "ds_read_b64_tr_b16 a[156:157], v123 offset:32768\n"
    "v_cvt_pk_bf16_f32 v67, v38, v39\n"
    "v_smfmac_f32_16x16x64_bf16 a[200:203], a[208:211], v[80:87], v126\n"
    "ds_read_b64_tr_b16 a[158:159], v123 offset:36864\n"
    "; -- A29a\n"
    "s_waitcnt lgkmcnt(7)\n"
    "v_mfma_f32_16x16x32_bf16 v[32:35], a[96:99], a[0:3], v[96:99]\n"
    "v_exp_f32 v48, v48\n"
    "v_mfma_f32_16x16x32_bf16 v[32:35], a[100:103], a[4:7], v[32:35]\n"
    "ds_read_b128 a[64:67], v112 offset:49152\n"
    "ds_read_b128 a[68:71], v113 offset:49152\n"
    "v_mfma_f32_16x16x32_bf16 v[32:35], a[104:107], a[8:11], v[32:35]\n"
    "v_exp_f32 v49, v49\n"
    "v_mfma_f32_16x16x32_bf16 v[32:35], a[108:111], a[12:15], v[32:35]\n"
    "ds_read_b128 a[72:75], v114 offset:49152\n"
    "v_cvt_pk_bf16_f32 v80, v48, v49\n"
    "v_mfma_f32_16x16x32_bf16 v[36:39], a[112:115], a[0:3], v[96:99]\n"
    "v_exp_f32 v50, v50\n"
    "v_mfma_f32_16x16x32_bf16 v[36:39], a[116:119], a[4:7], v[36:39]\n"
    "ds_read_b128 a[76:79], v115 offset:49152\n"
    "ds_read_b128 a[80:83], v112 offset:53248\n"
    "v_mfma_f32_16x16x32_bf16 v[36:39], a[120:123], a[8:11], v[36:39]\n"
    "v_exp_f32 v51, v51\n"
    "v_mfma_f32_16x16x32_bf16 v[36:39], a[124:127], a[12:15], v[36:39]\n"
    "ds_read_b128 a[84:87], v113 offset:53248\n"
    "v_cvt_pk_bf16_f32 v81, v50, v51\n"
    "v_mfma_f32_16x16x32_bf16 v[128:131], a[128:131], v[64:67], v[128:131]\n"
    "v_exp_f32 v52, v52\n"
    "v_mfma_f32_16x16x32_bf16 v[132:135], a[132:135], v[64:67], v[132:135]\n"
    "ds_read_b128 a[88:91], v114 offset:53248\n"
    "ds_read_b128 a[92:95], v115 offset:53248\n"
    "v_mfma_f32_16x16x32_bf16 v[136:139], a[136:139], v[64:67], v[136:139]\n"
    "v_exp_f32 v53, v53\n"
    "v_mfma_f32_16x16x32_bf16 v[140:143], a[140:143], v[64:67], v[140:143]\n"
    "ds_read_b64_tr_b16 a[160:161], v116 offset:40960\n"
    "v_cvt_pk_bf16_f32 v82, v52, v53\n"
    "s_waitcnt lgkmcnt(7)\n"
    "v_mfma_f32_16x16x32_bf16 v[144:147], a[144:147], v[64:67], v[144:147]\n"
    "v_exp_f32 v54, v54\n"
    "v_mfma_f32_16x16x32_bf16 v[148:151], a[148:151], v[64:67], v[148:151]\n"
    "ds_read_b64_tr_b16 a[162:163], v116 offset:45056\n"
    "ds_read_b64_tr_b16 a[164:165], v117 offset:40960\n"
    "v_mfma_f32_16x16x32_bf16 v[152:155], a[152:155], v[64:67], v[152:155]\n"
    "v_exp_f32 v55, v55\n"
    "v_mfma_f32_16x16x32_bf16 v[156:159], a[156:159], v[64:67], v[156:159]\n"
    "ds_read_b64_tr_b16 a[166:167], v117 offset:45056\n"
    "v_cvt_pk_bf16_f32 v83, v54, v55\n"
    "; -- A29b\n"
    "v_mfma_f32_16x16x32_bf16 v[48:51], a[96:99], a[32:35], v[104:107]\n"
    "v_exp_f32 v32, v32\n"
    "v_mfma_f32_16x16x32_bf16 v[48:51], a[100:103], a[36:39], v[48:51]\n"
    "ds_read_b64_tr_b16 a[168:169], v118 offset:40960\n"
    "ds_read_b64_tr_b16 a[170:171], v118 offset:45056\n"
    "v_mfma_f32_16x16x32_bf16 v[48:51], a[104:107], a[40:43], v[48:51]\n"
    "v_exp_f32 v33, v33\n"
    "v_mfma_f32_16x16x32_bf16 v[48:51], a[108:111], a[44:47], v[48:51]\n"
    "ds_read_b64_tr_b16 a[172:173], v119 offset:40960\n"
    "v_cvt_pk_bf16_f32 v68, v32, v33\n"
    "v_mfma_f32_16x16x32_bf16 v[52:55], a[112:115], a[32:35], v[104:107]\n"
    "v_exp_f32 v34, v34\n"
    "v_mfma_f32_16x16x32_bf16 v[52:55], a[116:119], a[36:39], v[52:55]\n"
    "ds_read_b64_tr_b16 a[174:175], v119 offset:45056\n"
    "ds_read_b64_tr_b16 a[176:177], v120 offset:40960\n"
    "v_mfma_f32_16x16x32_bf16 v[52:55], a[120:123], a[40:43], v[52:55]\n"
    "v_exp_f32 v35, v35\n"
    "v_mfma_f32_16x16x32_bf16 v[52:55], a[124:127], a[44:47], v[52:55]\n"
    "ds_read_b64_tr_b16 a[178:179], v120 offset:45056\n"
    "v_cvt_pk_bf16_f32 v69, v34, v35\n"
    "v_mfma_f32_16x16x32_bf16 v[192:195], a[128:131], v[80:83], v[192:195]\n"
    "v_exp_f32 v36, v36\n"
    "v_mfma_f32_16x16x32_bf16 v[196:199], a[132:135], v[80:83], v[196:199]\n"
    "ds_read_b64_tr_b16 a[180:181], v121 offset:40960\n"
    "ds_read_b64_tr_b16 a[182:183], v121 offset:45056\n"
    "v_mfma_f32_16x16x32_bf16 v[200:203], a[136:139], v[80:83], v[200:203]\n"
    "v_exp_f32 v37, v37\n"
    "v_mfma_f32_16x16x32_bf16 v[204:207], a[140:143], v[80:83], v[204:207]\n"
    "ds_read_b64_tr_b16 a[184:185], v122 offset:40960\n"
    "v_cvt_pk_bf16_f32 v70, v36, v37\n"
    "v_mfma_f32_16x16x32_bf16 v[208:211], a[144:147], v[80:83], v[208:211]\n"
    "v_exp_f32 v38, v38\n"
    "v_mfma_f32_16x16x32_bf16 v[212:215], a[148:151], v[80:83], v[212:215]\n"
    "ds_read_b64_tr_b16 a[186:187], v122 offset:45056\n"
    "ds_read_b64_tr_b16 a[188:189], v123 offset:40960\n"
    "v_mfma_f32_16x16x32_bf16 v[216:219], a[152:155], v[80:83], v[216:219]\n"
    "v_exp_f32 v39, v39\n"
    "v_mfma_f32_16x16x32_bf16 v[220:223], a[156:159], v[80:83], v[220:223]\n"
    "ds_read_b64_tr_b16 a[190:191], v123 offset:45056\n"
    "v_cvt_pk_bf16_f32 v71, v38, v39\n"
    "; -- A30a\n"
    "v_mfma_f32_16x16x32_bf16 v[32:35], a[64:67], a[0:3], v[96:99]\n"
    "v_exp_f32 v48, v48\n"
    "v_mfma_f32_16x16x32_bf16 v[32:35], a[68:71], a[4:7], v[32:35]\n"
    "s_waitcnt vmcnt(0)\n"
    "s_barrier\n"
    "s_waitcnt lgkmcnt(6)\n"
    "v_mfma_f32_16x16x32_bf16 v[32:35], a[72:75], a[8:11], v[32:35]\n"
    "v_exp_f32 v49, v49\n"
    "v_mfma_f32_16x16x32_bf16 v[32:35], a[76:79], a[12:15], v[32:35]\n"
    "ds_read_b128 a[96:99], v112 offset:57344\n"
    "v_cvt_pk_bf16_f32 v84, v48, v49\n"
    "v_mfma_f32_16x16x32_bf16 v[36:39], a[80:83], a[0:3], v[96:99]\n"
    "v_exp_f32 v50, v50\n"
    "v_mfma_f32_16x16x32_bf16 v[36:39], a[84:87], a[4:7], v[36:39]\n"
    "ds_read_b128 a[100:103], v113 offset:57344\n"
    "ds_read_b128 a[104:107], v114 offset:57344\n"
    "v_mfma_f32_16x16x32_bf16 v[36:39], a[88:91], a[8:11], v[36:39]\n"
    "v_exp_f32 v51, v51\n"
    "v_mfma_f32_16x16x32_bf16 v[36:39], a[92:95], a[12:15], v[36:39]\n"
    "ds_read_b128 a[108:111], v115 offset:57344\n"
    "v_cvt_pk_bf16_f32 v85, v50, v51\n"
    "v_mfma_f32_16x16x32_bf16 v[128:131], a[160:163], v[68:71], v[128:131]\n"
    "v_exp_f32 v52, v52\n"
    "v_mfma_f32_16x16x32_bf16 v[132:135], a[164:167], v[68:71], v[132:135]\n"
    "ds_read_b128 a[112:115], v112 offset:61440\n"
    "ds_read_b128 a[116:119], v113 offset:61440\n"
    "v_mfma_f32_16x16x32_bf16 v[136:139], a[168:171], v[68:71], v[136:139]\n"
    "v_exp_f32 v53, v53\n"
    "v_mfma_f32_16x16x32_bf16 v[140:143], a[172:175], v[68:71], v[140:143]\n"
    "ds_read_b128 a[120:123], v114 offset:61440\n"
    "v_cvt_pk_bf16_f32 v86, v52, v53\n"
    "v_mfma_f32_16x16x32_bf16 v[144:147], a[176:179], v[68:71], v[144:147]\n"
    "v_exp_f32 v54, v54\n"
    "s_waitcnt lgkmcnt(7)\n"
    "v_mfma_f32_16x16x32_bf16 v[148:151], a[180:183], v[68:71], v[148:151]\n"
    "ds_read_b128 a[124:127], v115 offset:61440\n"
    "ds_read_b64_tr_b16 a[128:129], v116 offset:49152\n"
    "v_mfma_f32_16x16x32_bf16 v[152:155], a[184:187], v[68:71], v[152:155]\n"
    "v_exp_f32 v55, v55\n"
    "v_mfma_f32_16x16x32_bf16 v[156:159], a[188:191], v[68:71], v[156:159]\n"
    "ds_read_b64_tr_b16 a[130:131], v116 offset:53248\n"
    "v_cvt_pk_bf16_f32 v87, v54, v55\n"
    "v_smfmac_f32_16x16x64_bf16 a[192:195], a[208:211], v[64:71], v126\n"
    "ds_read_b64_tr_b16 a[132:133], v117 offset:49152\n"
    "; -- A30b\n"
    "v_mfma_f32_16x16x32_bf16 v[48:51], a[64:67], a[32:35], v[104:107]\n"
    "v_exp_f32 v32, v32\n"
    "v_mfma_f32_16x16x32_bf16 v[48:51], a[68:71], a[36:39], v[48:51]\n"
    "ds_read_b64_tr_b16 a[134:135], v117 offset:53248\n"
    "ds_read_b64_tr_b16 a[136:137], v118 offset:49152\n"
    "v_mfma_f32_16x16x32_bf16 v[48:51], a[72:75], a[40:43], v[48:51]\n"
    "v_exp_f32 v33, v33\n"
    "v_mfma_f32_16x16x32_bf16 v[48:51], a[76:79], a[44:47], v[48:51]\n"
    "ds_read_b64_tr_b16 a[138:139], v118 offset:53248\n"
    "v_cvt_pk_bf16_f32 v64, v32, v33\n"
    "v_mfma_f32_16x16x32_bf16 v[52:55], a[80:83], a[32:35], v[104:107]\n"
    "v_exp_f32 v34, v34\n"
    "v_mfma_f32_16x16x32_bf16 v[52:55], a[84:87], a[36:39], v[52:55]\n"
    "ds_read_b64_tr_b16 a[140:141], v119 offset:49152\n"
    "ds_read_b64_tr_b16 a[142:143], v119 offset:53248\n"
    "v_mfma_f32_16x16x32_bf16 v[52:55], a[88:91], a[40:43], v[52:55]\n"
    "v_exp_f32 v35, v35\n"
    "v_mfma_f32_16x16x32_bf16 v[52:55], a[92:95], a[44:47], v[52:55]\n"
    "ds_read_b64_tr_b16 a[144:145], v120 offset:49152\n"
    "v_cvt_pk_bf16_f32 v65, v34, v35\n"
    "v_mfma_f32_16x16x32_bf16 v[192:195], a[160:163], v[84:87], v[192:195]\n"
    "v_exp_f32 v36, v36\n"
    "v_mfma_f32_16x16x32_bf16 v[196:199], a[164:167], v[84:87], v[196:199]\n"
    "ds_read_b64_tr_b16 a[146:147], v120 offset:53248\n"
    "ds_read_b64_tr_b16 a[148:149], v121 offset:49152\n"
    "v_mfma_f32_16x16x32_bf16 v[200:203], a[168:171], v[84:87], v[200:203]\n"
    "v_exp_f32 v37, v37\n"
    "v_mfma_f32_16x16x32_bf16 v[204:207], a[172:175], v[84:87], v[204:207]\n"
    "ds_read_b64_tr_b16 a[150:151], v121 offset:53248\n"
    "v_cvt_pk_bf16_f32 v66, v36, v37\n"
    "v_mfma_f32_16x16x32_bf16 v[208:211], a[176:179], v[84:87], v[208:211]\n"
    "v_exp_f32 v38, v38\n"
    "v_mfma_f32_16x16x32_bf16 v[212:215], a[180:183], v[84:87], v[212:215]\n"
    "ds_read_b64_tr_b16 a[152:153], v122 offset:49152\n"
    "ds_read_b64_tr_b16 a[154:155], v122 offset:53248\n"
    "v_mfma_f32_16x16x32_bf16 v[216:219], a[184:187], v[84:87], v[216:219]\n"
    "v_exp_f32 v39, v39\n"
    "v_mfma_f32_16x16x32_bf16 v[220:223], a[188:191], v[84:87], v[220:223]\n"
    "ds_read_b64_tr_b16 a[156:157], v123 offset:49152\n"
    "v_cvt_pk_bf16_f32 v67, v38, v39\n"
    "v_smfmac_f32_16x16x64_bf16 a[200:203], a[208:211], v[80:87], v126\n"
    "ds_read_b64_tr_b16 a[158:159], v123 offset:53248\n"
    "; -- A31a\n"
    "s_waitcnt lgkmcnt(7)\n"
    "v_mfma_f32_16x16x32_bf16 v[32:35], a[96:99], a[0:3], v[96:99]\n"
    "v_exp_f32 v48, v48\n"
    "v_mfma_f32_16x16x32_bf16 v[32:35], a[100:103], a[4:7], v[32:35]\n"
    "ds_read_b64_tr_b16 a[160:161], v116 offset:57344\n"
    "ds_read_b64_tr_b16 a[162:163], v116 offset:61440\n"
    "v_mfma_f32_16x16x32_bf16 v[32:35], a[104:107], a[8:11], v[32:35]\n"
    "v_exp_f32 v49, v49\n"
    "v_mfma_f32_16x16x32_bf16 v[32:35], a[108:111], a[12:15], v[32:35]\n"
    "ds_read_b64_tr_b16 a[164:165], v117 offset:57344\n"
    "v_cvt_pk_bf16_f32 v80, v48, v49\n"
    "v_mfma_f32_16x16x32_bf16 v[36:39], a[112:115], a[0:3], v[96:99]\n"
    "v_exp_f32 v50, v50\n"
    "v_mfma_f32_16x16x32_bf16 v[36:39], a[116:119], a[4:7], v[36:39]\n"
    "ds_read_b64_tr_b16 a[166:167], v117 offset:61440\n"
    "ds_read_b64_tr_b16 a[168:169], v118 offset:57344\n"
    "v_mfma_f32_16x16x32_bf16 v[36:39], a[120:123], a[8:11], v[36:39]\n"
    "v_exp_f32 v51, v51\n"
    "v_mfma_f32_16x16x32_bf16 v[36:39], a[124:127], a[12:15], v[36:39]\n"
    "ds_read_b64_tr_b16 a[170:171], v118 offset:61440\n"
    "v_cvt_pk_bf16_f32 v81, v50, v51\n"
    "v_mfma_f32_16x16x32_bf16 v[128:131], a[128:131], v[64:67], v[128:131]\n"
    "v_exp_f32 v52, v52\n"
    "v_mfma_f32_16x16x32_bf16 v[132:135], a[132:135], v[64:67], v[132:135]\n"
    "ds_read_b64_tr_b16 a[172:173], v119 offset:57344\n"
    "ds_read_b64_tr_b16 a[174:175], v119 offset:61440\n"
    "v_mfma_f32_16x16x32_bf16 v[136:139], a[136:139], v[64:67], v[136:139]\n"
    "v_exp_f32 v53, v53\n"
    "v_mfma_f32_16x16x32_bf16 v[140:143], a[140:143], v[64:67], v[140:143]\n"
    "ds_read_b64_tr_b16 a[176:177], v120 offset:57344\n"
    "v_cvt_pk_bf16_f32 v82, v52, v53\n"
    "s_waitcnt lgkmcnt(7)\n"
    "v_mfma_f32_16x16x32_bf16 v[144:147], a[144:147], v[64:67], v[144:147]\n"
    "v_exp_f32 v54, v54\n"
    "v_mfma_f32_16x16x32_bf16 v[148:151], a[148:151], v[64:67], v[148:151]\n"
    "ds_read_b64_tr_b16 a[178:179], v120 offset:61440\n"
    "ds_read_b64_tr_b16 a[180:181], v121 offset:57344\n"
    "v_mfma_f32_16x16x32_bf16 v[152:155], a[152:155], v[64:67], v[152:155]\n"
    "v_exp_f32 v55, v55\n"
    "v_mfma_f32_16x16x32_bf16 v[156:159], a[156:159], v[64:67], v[156:159]\n"
    "ds_read_b64_tr_b16 a[182:183], v121 offset:61440\n"
    "v_cvt_pk_bf16_f32 v83, v54, v55\n"
    "; -- A31b\n"
    "v_mfma_f32_16x16x32_bf16 v[48:51], a[96:99], a[32:35], v[104:107]\n"
    "v_exp_f32 v32, v32\n"
    "v_mfma_f32_16x16x32_bf16 v[48:51], a[100:103], a[36:39], v[48:51]\n"
    "ds_read_b64_tr_b16 a[184:185], v122 offset:57344\n"
    "ds_read_b64_tr_b16 a[186:187], v122 offset:61440\n"
    "v_mfma_f32_16x16x32_bf16 v[48:51], a[104:107], a[40:43], v[48:51]\n"
    "v_exp_f32 v33, v33\n"
    "v_mfma_f32_16x16x32_bf16 v[48:51], a[108:111], a[44:47], v[48:51]\n"
    "ds_read_b64_tr_b16 a[188:189], v123 offset:57344\n"
    "v_cvt_pk_bf16_f32 v68, v32, v33\n"
    "v_mfma_f32_16x16x32_bf16 v[52:55], a[112:115], a[32:35], v[104:107]\n"
    "v_exp_f32 v34, v34\n"
    "v_mfma_f32_16x16x32_bf16 v[52:55], a[116:119], a[36:39], v[52:55]\n"
    "ds_read_b64_tr_b16 a[190:191], v123 offset:61440\n"
    "v_mfma_f32_16x16x32_bf16 v[52:55], a[120:123], a[40:43], v[52:55]\n"
    "v_exp_f32 v35, v35\n"
    "v_mfma_f32_16x16x32_bf16 v[52:55], a[124:127], a[44:47], v[52:55]\n"
    "v_cvt_pk_bf16_f32 v69, v34, v35\n"
    "v_mfma_f32_16x16x32_bf16 v[192:195], a[128:131], v[80:83], v[192:195]\n"
    "v_exp_f32 v36, v36\n"
    "v_mfma_f32_16x16x32_bf16 v[196:199], a[132:135], v[80:83], v[196:199]\n"
    "v_mfma_f32_16x16x32_bf16 v[200:203], a[136:139], v[80:83], v[200:203]\n"
    "v_exp_f32 v37, v37\n"
    "v_mfma_f32_16x16x32_bf16 v[204:207], a[140:143], v[80:83], v[204:207]\n"
    "v_cvt_pk_bf16_f32 v70, v36, v37\n"
    "v_mfma_f32_16x16x32_bf16 v[208:211], a[144:147], v[80:83], v[208:211]\n"
    "v_exp_f32 v38, v38\n"
    "v_mfma_f32_16x16x32_bf16 v[212:215], a[148:151], v[80:83], v[212:215]\n"
    "v_mfma_f32_16x16x32_bf16 v[216:219], a[152:155], v[80:83], v[216:219]\n"
    "v_exp_f32 v39, v39\n"
    "v_mfma_f32_16x16x32_bf16 v[220:223], a[156:159], v[80:83], v[220:223]\n"
    "v_cvt_pk_bf16_f32 v71, v38, v39\n"
    "; -- X1\n"
    "s_nop 1\n"
    "v_mfma_f32_16x16x32_bf16 v[128:131], a[160:163], v[68:71], v[128:131]\n"
    "v_exp_f32 v48, v48\n"
    "s_waitcnt lgkmcnt(1)\n"
    "v_mfma_f32_16x16x32_bf16 v[132:135], a[164:167], v[68:71], v[132:135]\n"
    "v_mfma_f32_16x16x32_bf16 v[136:139], a[168:171], v[68:71], v[136:139]\n"
    "v_exp_f32 v49, v49\n"
    "v_mfma_f32_16x16x32_bf16 v[140:143], a[172:175], v[68:71], v[140:143]\n"
    "v_cvt_pk_bf16_f32 v84, v48, v49\n"
    "v_mfma_f32_16x16x32_bf16 v[144:147], a[176:179], v[68:71], v[144:147]\n"
    "v_exp_f32 v50, v50\n"
    "v_mfma_f32_16x16x32_bf16 v[148:151], a[180:183], v[68:71], v[148:151]\n"
    "v_mfma_f32_16x16x32_bf16 v[152:155], a[184:187], v[68:71], v[152:155]\n"
    "v_exp_f32 v51, v51\n"
    "s_waitcnt lgkmcnt(0)\n"
    "v_mfma_f32_16x16x32_bf16 v[156:159], a[188:191], v[68:71], v[156:159]\n"
    "v_cvt_pk_bf16_f32 v85, v50, v51\n"
    "v_smfmac_f32_16x16x64_bf16 a[192:195], a[208:211], v[64:71], v126\n"
    "v_exp_f32 v52, v52\n"
    "v_mfma_f32_16x16x32_bf16 v[40:43], a[64:67], a[16:19], v[100:103]\n"
    "v_mfma_f32_16x16x32_bf16 v[40:43], a[68:71], a[20:23], v[40:43]\n"
    "v_exp_f32 v53, v53\n"
    "v_mfma_f32_16x16x32_bf16 v[40:43], a[72:75], a[24:27], v[40:43]\n"
    "v_cvt_pk_bf16_f32 v86, v52, v53\n"
    "v_mfma_f32_16x16x32_bf16 v[40:43], a[76:79], a[28:31], v[40:43]\n"
    "v_exp_f32 v54, v54\n"
    "v_mfma_f32_16x16x32_bf16 v[44:47], a[80:83], a[16:19], v[100:103]\n"
    "v_mfma_f32_16x16x32_bf16 v[44:47], a[84:87], a[20:23], v[44:47]\n"
    "v_exp_f32 v55, v55\n"
    "v_mfma_f32_16x16x32_bf16 v[44:47], a[88:91], a[24:27], v[44:47]\n"
    "v_cvt_pk_bf16_f32 v87, v54, v55\n"
    "v_mfma_f32_16x16x32_bf16 v[44:47], a[92:95], a[28:31], v[44:47]\n"
    "; -- B0\n"
    "s_nop 0\n"
    "v_mfma_f32_16x16x32_bf16 v[192:195], a[160:163], v[84:87], v[192:195]\n"
    "v_exp_f32 v40, v40\n"
    "v_mfma_f32_16x16x32_bf16 v[196:199], a[164:167], v[84:87], v[196:199]\n"
    "s_add_u32 s82, %[pb0], 0\n"
    "v_cvt_pk_bf16_f32 v32, v128, v129\n"
    "v_mfma_f32_16x16x32_bf16 v[200:203], a[168:171], v[84:87], v[200:203]\n"
    "v_exp_f32 v41, v41\n"
    "v_mfma_f32_16x16x32_bf16 v[204:207], a[172:175], v[84:87], v[204:207]\n"
    "v_cvt_pk_bf16_f32 v33, v130, v131\n"
    "v_cvt_pk_bf16_f32 v72, v40, v41\n"
    "v_mfma_f32_16x16x32_bf16 v[208:211], a[176:179], v[84:87], v[208:211]\n"
    "v_exp_f32 v42, v42\n"
    "v_mfma_f32_16x16x32_bf16 v[212:215], a[180:183], v[84:87], v[212:215]\n"
    "v_cvt_pk_bf16_f32 v34, v132, v133\n"
    "v_cvt_pk_bf16_f32 v35, v134, v135\n"
    "v_mfma_f32_16x16x32_bf16 v[216:219], a[184:187], v[84:87], v[216:219]\n"
    "v_exp_f32 v43, v43\n"
    "v_mfma_f32_16x16x32_bf16 v[220:223], a[188:191], v[84:87], v[220:223]\n"
    "buffer_store_dwordx4 v[32:35], v127, %[srdw], s82 offen sc1\n"
    "v_cvt_pk_bf16_f32 v73, v42, v43\n"
    "v_smfmac_f32_16x16x64_bf16 a[200:203], a[208:211], v[80:87], v126\n"
    "v_exp_f32 v44, v44\n"
    "v_mfma_f32_16x16x32_bf16 v[56:59], a[64:67], a[48:51], v[108:111]\n"
    "v_cvt_pk_bf16_f32 v36, v136, v137\n"
    "v_cvt_pk_bf16_f32 v37, v138, v139\n"
    "v_mfma_f32_16x16x32_bf16 v[56:59], a[68:71], a[52:55], v[56:59]\n"
    "v_exp_f32 v45, v45\n"
    "v_mfma_f32_16x16x32_bf16 v[56:59], a[72:75], a[56:59], v[56:59]\n"
    "v_cvt_pk_bf16_f32 v38, v140, v141\n"
    "v_cvt_pk_bf16_f32 v74, v44, v45\n"
    "v_mfma_f32_16x16x32_bf16 v[56:59], a[76:79], a[60:63], v[56:59]\n"
    "v_exp_f32 v46, v46\n"
    "v_mfma_f32_16x16x32_bf16 v[60:63], a[80:83], a[48:51], v[108:111]\n"
    "v_cvt_pk_bf16_f32 v39, v142, v143\n"
    "v_mfma_f32_16x16x32_bf16 v[60:63], a[84:87], a[52:55], v[60:63]\n"
    "v_exp_f32 v47, v47\n"
    "v_mfma_f32_16x16x32_bf16 v[60:63], a[88:91], a[56:59], v[60:63]\n"
    "buffer_store_dwordx4 v[36:39], v127, %[srdw], s82 offen offset:1024 sc1\n"
    "v_cvt_pk_bf16_f32 v75, v46, v47\n"
    "v_mfma_f32_16x16x32_bf16 v[60:63], a[92:95], a[60:63], v[60:63]\n"
    "v_cvt_pk_bf16_f32 v48, v144, v145\n"
    "v_cvt_pk_bf16_f32 v49, v146, v147\n"
    "v_cvt_pk_bf16_f32 v50, v148, v149\n"
    "v_cvt_pk_bf16_f32 v51, v150, v151\n"
    "buffer_store_dwordx4 v[48:51], v127, %[srdw], s82 offen offset:2048 sc1\n"
    "v_cvt_pk_bf16_f32 v52, v152, v153\n"
    "v_cvt_pk_bf16_f32 v53, v154, v155\n"
    "v_cvt_pk_bf16_f32 v54, v156, v157\n"
    "v_cvt_pk_bf16_f32 v55, v158, v159\n"
    "buffer_store_dwordx4 v[52:55], v127, %[srdw], s82 offen offset:3072 sc1\n"
    "; -- B1\n"
    "v_mfma_f32_16x16x32_bf16 v[40:43], a[96:99], a[16:19], v[100:103]\n"
    "v_exp_f32 v56, v56\n"
    "v_mfma_f32_16x16x32_bf16 v[40:43], a[100:103], a[20:23], v[40:43]\n"
    "s_add_u32 s82, %[pb1], 0\n"
    "v_cvt_pk_bf16_f32 v64, v192, v193\n"
    "v_mfma_f32_16x16x32_bf16 v[40:43], a[104:107], a[24:27], v[40:43]\n"
    "v_exp_f32 v57, v57\n"
    "v_mfma_f32_16x16x32_bf16 v[40:43], a[108:111], a[28:31], v[40:43]\n"
    "v_cvt_pk_bf16_f32 v65, v194, v195\n"
    "v_cvt_pk_bf16_f32 v88, v56, v57\n"
    "v_mfma_f32_16x16x32_bf16 v[44:47], a[112:115], a[16:19], v[100:103]\n"
    "v_exp_f32 v58, v58\n"
    "v_mfma_f32_16x16x32_bf16 v[44:47], a[116:119], a[20:23], v[44:47]\n"
    "v_cvt_pk_bf16_f32 v66, v196, v197\n"
    "v_cvt_pk_bf16_f32 v67, v198, v199\n"
    "v_mfma_f32_16x16x32_bf16 v[44:47], a[120:123], a[24:27], v[44:47]\n"
    "v_exp_f32 v59, v59\n"
    "v_mfma_f32_16x16x32_bf16 v[44:47], a[124:127], a[28:31], v[44:47]\n"
    "buffer_store_dwordx4 v[64:67], v127, %[srdw], s82 offen sc1\n"
    "v_cvt_pk_bf16_f32 v89, v58, v59\n"
    "v_mfma_f32_16x16x32_bf16 v[160:163], a[128:131], v[72:75], v[160:163]\n"
    "v_exp_f32 v60, v60\n"
    "v_mfma_f32_16x16x32_bf16 v[164:167], a[132:135], v[72:75], v[164:167]\n"
    "v_cvt_pk_bf16_f32 v68, v200, v201\n"
    "v_cvt_pk_bf16_f32 v69, v202, v203\n"
    "v_mfma_f32_16x16x32_bf16 v[168:171], a[136:139], v[72:75], v[168:171]\n"
    "v_exp_f32 v61, v61\n"
    "v_mfma_f32_16x16x32_bf16 v[172:175], a[140:143], v[72:75], v[172:175]\n"
    "v_cvt_pk_bf16_f32 v70, v204, v205\n"
    "v_cvt_pk_bf16_f32 v90, v60, v61\n"
    "v_mfma_f32_16x16x32_bf16 v[176:179], a[144:147], v[72:75], v[176:179]\n"
    "v_exp_f32 v62, v62\n"
    "v_mfma_f32_16x16x32_bf16 v[180:183], a[148:151], v[72:75], v[180:183]\n"
    "v_cvt_pk_bf16_f32 v71, v206, v207\n"
    "v_mfma_f32_16x16x32_bf16 v[184:187], a[152:155], v[72:75], v[184:187]\n"
    "v_exp_f32 v63, v63\n"
    "v_mfma_f32_16x16x32_bf16 v[188:191], a[156:159], v[72:75], v[188:191]\n"
    "buffer_store_dwordx4 v[68:71], v127, %[srdw], s82 offen offset:1024 sc1\n"
    "v_cvt_pk_bf16_f32 v91, v62, v63\n"
    "v_cvt_pk_bf16_f32 v32, v208, v209\n"
    "v_cvt_pk_bf16_f32 v33, v210, v211\n"
    "v_cvt_pk_bf16_f32 v34, v212, v213\n"
    "v_cvt_pk_bf16_f32 v35, v214, v215\n"
    "buffer_store_dwordx4 v[32:35], v127, %[srdw], s82 offen offset:2048 sc1\n"
    "v_cvt_pk_bf16_f32 v36, v216, v217\n"
    "v_cvt_pk_bf16_f32 v37, v218, v219\n"
    "v_cvt_pk_bf16_f32 v38, v220, v221\n"
    "v_cvt_pk_bf16_f32 v39, v222, v223\n"
    "buffer_store_dwordx4 v[36:39], v127, %[srdw], s82 offen offset:3072 sc1\n"
    "v_accvgpr_read_b32 v28, a192\n"
    "v_accvgpr_read_b32 v29, a193\n"
    "v_accvgpr_read_b32 v30, a200\n"
    "v_accvgpr_read_b32 v31, a201\n"
    "v_add_f32 v28, v28, v29\n"
    "v_add_f32 v30, v30, v31\n"
    "v_cndmask_b32 v29, v28, v30, s[86:87]\n"
    "v_cndmask_b32 v28, %[m0], %[m2], s[86:87]\n"
    "v_cmp_nlt_f32 vcc, v29, v20\n"
    "s_mov_b32 exec_hi, 0\nbuffer_store_dwordx2 v[28:29], v21, %[srdw], %[sbase] offen sc1\ns_mov_b32 exec_hi, -1\n"
    "ds_write_b64 v19, v[28:29] offset:0\n"
    "s_cmp_lg_u64 vcc, 0\n"
    "s_cselect_b32 s83, 1, 0\n"
    "s_or_b32 %[bad], %[bad], s83\n"
    "; -- B2\n"
    "v_mfma_f32_16x16x32_bf16 v[56:59], a[96:99], a[48:51], v[108:111]\n"
    "v_exp_f32 v40, v40\n"
    "v_mfma_f32_16x16x32_bf16 v[56:59], a[100:103], a[52:55], v[56:59]\n"
    "ds_read_b128 a[64:67], v112 offset:0\n"
    "ds_read_b128 a[68:71], v113 offset:0\n"
    "v_mfma_f32_16x16x32_bf16 v[56:59], a[104:107], a[56:59], v[56:59]\n"
    "v_exp_f32 v41, v41\n"
    "v_mfma_f32_16x16x32_bf16 v[56:59], a[108:111], a[60:63], v[56:59]\n"
    "ds_read_b128 a[72:75], v114 offset:0\n"
    "v_cvt_pk_bf16_f32 v76, v40, v41\n"
    "v_mfma_f32_16x16x32_bf16 v[60:63], a[112:115], a[48:51], v[108:111]\n"
    "v_exp_f32 v42, v42\n"
    "v_mfma_f32_16x16x32_bf16 v[60:63], a[116:119], a[52:55], v[60:63]\n"
    "ds_read_b128 a[76:79], v115 offset:0\n"
    "ds_read_b128 a[80:83], v112 offset:4096\n"
    "v_mfma_f32_16x16x32_bf16 v[60:63], a[120:123], a[56:59], v[60:63]\n"
    "v_exp_f32 v43, v43\n"
    "v_mfma_f32_16x16x32_bf16 v[60:63], a[124:127], a[60:63], v[60:63]\n"
    "ds_read_b128 a[84:87], v113 offset:4096\n"
    "v_cvt_pk_bf16_f32 v77, v42, v43\n"
    "v_mfma_f32_16x16x32_bf16 v[224:227], a[128:131], v[88:91], v[224:227]\n"
    "v_exp_f32 v44, v44\n"
    "v_mfma_f32_16x16x32_bf16 v[228:231], a[132:135], v[88:91], v[228:231]\n"
    "ds_read_b128 a[88:91], v114 offset:4096\n"
    "ds_read_b128 a[92:95], v115 offset:4096\n"
    "v_mfma_f32_16x16x32_bf16 v[232:235], a[136:139], v[88:91], v[232:235]\n"
    "v_exp_f32 v45, v45\n"
    "v_mfma_f32_16x16x32_bf16 v[236:239], a[140:143], v[88:91], v[236:239]\n"
    "v_cvt_pk_bf16_f32 v78, v44, v45\n"
    "v_mfma_f32_16x16x32_bf16 v[240:243], a[144:147], v[88:91], v[240:243]\n"
    "v_exp_f32 v46, v46\n"
    "v_mfma_f32_16x16x32_bf16 v[244:247], a[148:151], v[88:91], v[244:247]\n"
    "v_mfma_f32_16x16x32_bf16 v[248:251], a[152:155], v[88:91], v[248:251]\n"
    "v_exp_f32 v47, v47\n"
    "v_mfma_f32_16x16x32_bf16 v[252:255], a[156:159], v[88:91], v[252:255]\n"
    "v_cvt_pk_bf16_f32 v79, v46, v47\n"
    "; -- B3\n"
    "s_waitcnt lgkmcnt(5)\n"
    "v_mfma_f32_16x16x32_bf16 v[40:43], a[64:67], a[16:19], v[100:103]\n"
    "v_exp_f32 v56, v56\n"
    "v_mfma_f32_16x16x32_bf16 v[40:43], a[68:71], a[20:23], v[40:43]\n"
    "ds_read_b128 a[96:99], v112 offset:8192\n"
    "ds_read_b128 a[100:103], v113 offset:8192\n"
    "v_mfma_f32_16x16x32_bf16 v[40:43], a[72:75], a[24:27], v[40:43]\n"
    "v_exp_f32 v57, v57\n"
    "s_waitcnt lgkmcnt(4)\n"
    "v_mfma_f32_16x16x32_bf16 v[40:43], a[76:79], a[28:31], v[40:43]\n"
    "ds_read_b128 a[104:107], v114 offset:8192\n"
    "v_cvt_pk_bf16_f32 v92, v56, v57\n"
    "v_mfma_f32_16x16x32_bf16 v[44:47], a[80:83], a[16:19], v[100:103]\n"
    "v_exp_f32 v58, v58\n"
    "v_mfma_f32_16x16x32_bf16 v[44:47], a[84:87], a[20:23], v[44:47]\n"
    "ds_read_b128 a[108:111], v115 offset:8192\n"
    "ds_read_b128 a[112:115], v112 offset:12288\n"
    "s_waitcnt lgkmcnt(5)\n"
    "v_mfma_f32_16x16x32_bf16 v[44:47], a[88:91], a[24:27], v[44:47]\n"
    "v_exp_f32 v59, v59\n"
    "v_mfma_f32_16x16x32_bf16 v[44:47], a[92:95], a[28:31], v[44:47]\n"
    "ds_read_b128 a[116:119], v113 offset:12288\n"
    "v_cvt_pk_bf16_f32 v93, v58, v59\n"
    "v_mfma_f32_16x16x32_bf16 v[160:163], a[160:163], v[76:79], v[160:163]\n"
    "v_exp_f32 v60, v60\n"
    "v_mfma_f32_16x16x32_bf16 v[164:167], a[164:167], v[76:79], v[164:167]\n"
    "ds_read_b128 a[120:123], v114 offset:12288\n"
    "ds_read_b128 a[124:127], v115 offset:12288\n"
    "v_mfma_f32_16x16x32_bf16 v[168:171], a[168:171], v[76:79], v[168:171]\n"
    "v_exp_f32 v61, v61\n"
    "v_mfma_f32_16x16x32_bf16 v[172:175], a[172:175], v[76:79], v[172:175]\n"
    "ds_read_b64_tr_b16 a[128:129], v116 offset:0\n"
    "v_cvt_pk_bf16_f32 v94, v60, v61\n"
    "v_mfma_f32_16x16x32_bf16 v[176:179], a[176:179], v[76:79], v[176:179]\n"
    "v_exp_f32 v62, v62\n"
    "v_mfma_f32_16x16x32_bf16 v[180:183], a[180:183], v[76:79], v[180:183]\n"
    "ds_read_b64_tr_b16 a[130:131], v116 offset:4096\n"
    "ds_read_b64_tr_b16 a[132:133], v117 offset:0\n"
    "v_mfma_f32_16x16x32_bf16 v[184:187], a[184:187], v[76:79], v[184:187]\n"
    "v_exp_f32 v63, v63\n"
    "v_mfma_f32_16x16x32_bf16 v[188:191], a[188:191], v[76:79], v[188:191]\n"
    "ds_read_b64_tr_b16 a[134:135], v117 offset:4096\n"
    "v_cvt_pk_bf16_f32 v95, v62, v63\n"
    "v_smfmac_f32_16x16x64_bf16 a[196:199], a[208:211], v[72:79], v126\n"
    "; -- B4\n"
    "v_mfma_f32_16x16x32_bf16 v[56:59], a[64:67], a[48:51], v[108:111]\n"
    "v_exp_f32 v40, v40\n"
    "v_mfma_f32_16x16x32_bf16 v[56:59], a[68:71], a[52:55], v[56:59]\n"
    "ds_read_b64_tr_b16 a[136:137], v118 offset:0\n"
    "ds_read_b64_tr_b16 a[138:139], v118 offset:4096\n"
    "v_mfma_f32_16x16x32_bf16 v[56:59], a[72:75], a[56:59], v[56:59]\n"
    "v_exp_f32 v41, v41\n"
    "v_mfma_f32_16x16x32_bf16 v[56:59], a[76:79], a[60:63], v[56:59]\n"
    "ds_read_b64_tr_b16 a[140:141], v119 offset:0\n"
    "v_cvt_pk_bf16_f32 v72, v40, v41\n"
    "v_mfma_f32_16x16x32_bf16 v[60:63], a[80:83], a[48:51], v[108:111]\n"
    "v_exp_f32 v42, v42\n"
    "v_mfma_f32_16x16x32_bf16 v[60:63], a[84:87], a[52:55], v[60:63]\n"
    "ds_read_b64_tr_b16 a[142:143], v119 offset:4096\n"
    "ds_read_b64_tr_b16 a[144:145], v120 offset:0\n"
    "v_mfma_f32_16x16x32_bf16 v[60:63], a[88:91], a[56:59], v[60:63]\n"
    "v_exp_f32 v43, v43\n"
    "v_mfma_f32_16x16x32_bf16 v[60:63], a[92:95], a[60:63], v[60:63]\n"
    "ds_read_b64_tr_b16 a[146:147], v120 offset:4096\n"
    "v_cvt_pk_bf16_f32 v73, v42, v43\n"
    "v_mfma_f32_16x16x32_bf16 v[224:227], a[160:163], v[92:95], v[224:227]\n"
    "v_exp_f32 v44, v44\n"
    "v_mfma_f32_16x16x32_bf16 v[228:231], a[164:167], v[92:95], v[228:231]\n"
    "ds_read_b64_tr_b16 a[148:149], v121 offset:0\n"
    "ds_read_b64_tr_b16 a[150:151], v121 offset:4096\n"
    "v_mfma_f32_16x16x32_bf16 v[232:235], a[168:171], v[92:95], v[232:235]\n"
    "v_exp_f32 v45, v45\n"
    "v_mfma_f32_16x16x32_bf16 v[236:239], a[172:175], v[92:95], v[236:239]\n"
    "ds_read_b64_tr_b16 a[152:153], v122 offset:0\n"
    "v_cvt_pk_bf16_f32 v74, v44, v45\n"
    "v_mfma_f32_16x16x32_bf16 v[240:243], a[176:179], v[92:95], v[240:243]\n"
    "v_exp_f32 v46, v46\n"
    "v_mfma_f32_16x16x32_bf16 v[244:247], a[180:183], v[92:95], v[244:247]\n"
    "ds_read_b64_tr_b16 a[154:155], v122 offset:4096\n"
    "ds_read_b64_tr_b16 a[156:157], v123 offset:0\n"
    "v_mfma_f32_16x16x32_bf16 v[248:251], a[184:187], v[92:95], v[248:251]\n"
    "v_exp_f32 v47, v47\n"
    "v_mfma_f32_16x16x32_bf16 v[252:255], a[188:191], v[92:95], v[252:255]\n"
    "ds_read_b64_tr_b16 a[158:159], v123 offset:4096\n"
    "v_cvt_pk_bf16_f32 v75, v46, v47\n"
    "v_smfmac_f32_16x16x64_bf16 a[204:207], a[208:211], v[88:95], v126\n"
    "s_waitcnt vmcnt(0)\n"
    "s_and_b32 s83, %[bad], s78\n"
    "s_bitcmp1_b32 s83, 0\n"
    "s_cselect_b32 s83, 0x40000000, %[sbase]\n"
    "s_mov_b64 exec, 0x10001\nbuffer_store_dwordx2 v[26:27], v18, %[srdw], s83 offen offset:256 sc1\ns_mov_b64 exec, -1\n"
    "; -- B5\n"
    "s_waitcnt lgkmcnt(6)\n"
    "v_mfma_f32_16x16x32_bf16 v[40:43], a[96:99], a[16:19], v[100:103]\n"
    "v_exp_f32 v56, v56\n"
    "v_mfma_f32_16x16x32_bf16 v[40:43], a[100:103], a[20:23], v[40:43]\n"
    "ds_read_b128 a[64:67], v112 offset:16384\n"
    "ds_read_b128 a[68:71], v113 offset:16384\n"
    "v_mfma_f32_16x16x32_bf16 v[40:43], a[104:107], a[24:27], v[40:43]\n"
    "v_exp_f32 v57, v57\n"
    "v_mfma_f32_16x16x32_bf16 v[40:43], a[108:111], a[28:31], v[40:43]\n"
    "ds_read_b128 a[72:75], v114 offset:16384\n"
    "v_cvt_pk_bf16_f32 v88, v56, v57\n"
    "v_mfma_f32_16x16x32_bf16 v[44:47], a[112:115], a[16:19], v[100:103]\n"
    "v_exp_f32 v58, v58\n"
    "v_mfma_f32_16x16x32_bf16 v[44:47], a[116:119], a[20:23], v[44:47]\n"
    "ds_read_b128 a[76:79], v115 offset:16384\n"
    "ds_read_b128 a[80:83], v112 offset:20480\n"
    "v_mfma_f32_16x16x32_bf16 v[44:47], a[120:123], a[24:27], v[44:47]\n"
    "v_exp_f32 v59, v59\n"
    "v_mfma_f32_16x16x32_bf16 v[44:47], a[124:127], a[28:31], v[44:47]\n"
    "Lh9_%=:\n"
    "s_cbranch_execz Lr9_%=\n"
    "ds_read_b128 a[84:87], v113 offset:20480\n"
    "v_cvt_pk_bf16_f32 v89, v58, v59\n"
    "v_mfma_f32_16x16x32_bf16 v[160:163], a[128:131], v[72:75], v[160:163]\n"
    "v_exp_f32 v60, v60\n"
    "v_mfma_f32_16x16x32_bf16 v[164:167], a[132:135], v[72:75], v[164:167]\n"
    "ds_read_b128 a[88:91], v114 offset:20480\n"
    "ds_read_b128 a[92:95], v115 offset:20480\n"
    "v_mfma_f32_16x16x32_bf16 v[168:171], a[136:139], v[72:75], v[168:171]\n"
    "v_exp_f32 v61, v61\n"
    "v_mfma_f32_16x16x32_bf16 v[172:175], a[140:143], v[72:75], v[172:175]\n"
    "ds_read_b64_tr_b16 a[160:161], v116 offset:8192\n"
    "v_cvt_pk_bf16_f32 v90, v60, v61\n"
    "v_mfma_f32_16x16x32_bf16 v[176:179], a[144:147], v[72:75], v[176:179]\n"
    "v_exp_f32 v62, v62\n"
    "s_waitcnt lgkmcnt(7)\n"
    "v_mfma_f32_16x16x32_bf16 v[180:183], a[148:151], v[72:75], v[180:183]\n"
    "ds_read_b64_tr_b16 a[162:163], v116 offset:12288\n"
    "ds_read_b64_tr_b16 a[164:165], v117 offset:8192\n"
    "v_mfma_f32_16x16x32_bf16 v[184:187], a[152:155], v[72:75], v[184:187]\n"
    "v_exp_f32 v63, v63\n"
    "v_mfma_f32_16x16x32_bf16 v[188:191], a[156:159], v[72:75], v[188:191]\n"
    "ds_read_b64_tr_b16 a[166:167], v117 offset:12288\n"
    "v_cvt_pk_bf16_f32 v91, v62, v63\n"
    "; -- B6\n"
    "v_mfma_f32_16x16x32_bf16 v[56:59], a[96:99], a[48:51], v[108:111]\n"
    "v_exp_f32 v40, v40\n"
    "v_mfma_f32_16x16x32_bf16 v[56:59], a[100:103], a[52:55], v[56:59]\n"
    "ds_read_b64_tr_b16 a[168:169], v118 offset:8192\n"
    "ds_read_b64_tr_b16 a[170:171], v118 offset:12288\n"
    "v_mfma_f32_16x16x32_bf16 v[56:59], a[104:107], a[56:59], v[56:59]\n"
    "v_exp_f32 v41, v41\n"
    "v_mfma_f32_16x16x32_bf16 v[56:59], a[108:111], a[60:63], v[56:59]\n"
    "ds_read_b64_tr_b16 a[172:173], v119 offset:8192\n"
    "v_cvt_pk_bf16_f32 v76, v40, v41\n"
    "v_mfma_f32_16x16x32_bf16 v[60:63], a[112:115], a[48:51], v[108:111]\n"
    "v_exp_f32 v42, v42\n"
    "v_mfma_f32_16x16x32_bf16 v[60:63], a[116:119], a[52:55], v[60:63]\n"
    "ds_read_b64_tr_b16 a[174:175], v119 offset:12288\n"
    "ds_read_b64_tr_b16 a[176:177], v120 offset:8192\n"
    "v_mfma_f32_16x16x32_bf16 v[60:63], a[120:123], a[56:59], v[60:63]\n"
    "v_exp_f32 v43, v43\n"
    "v_mfma_f32_16x16x32_bf16 v[60:63], a[124:127], a[60:63], v[60:63]\n"
    "ds_read_b64_tr_b16 a[178:179], v120 offset:12288\n"
    "v_cvt_pk_bf16_f32 v77, v42, v43\n"
    "v_mfma_f32_16x16x32_bf16 v[224:227], a[128:131], v[88:91], v[224:227]\n"
    "v_exp_f32 v44, v44\n"
    "v_mfma_f32_16x16x32_bf16 v[228:231], a[132:135], v[88:91], v[228:231]\n"
    "ds_read_b64_tr_b16 a[180:181], v121 offset:8192\n"
    "ds_read_b64_tr_b16 a[182:183], v121 offset:12288\n"
    "v_mfma_f32_16x16x32_bf16 v[232:235], a[136:139], v[88:91], v[232:235]\n"
    "v_exp_f32 v45, v45\n"
    "v_mfma_f32_16x16x32_bf16 v[236:239], a[140:143], v[88:91], v[236:239]\n"
    "ds_read_b64_tr_b16 a[184:185], v122 offset:8192\n"
    "v_cvt_pk_bf16_f32 v78, v44, v45\n"
    "v_mfma_f32_16x16x32_bf16 v[240:243], a[144:147], v[88:91], v[240:243]\n"
    "v_exp_f32 v46, v46\n"
    "v_mfma_f32_16x16x32_bf16 v[244:247], a[148:151], v[88:91], v[244:247]\n"
    "ds_read_b64_tr_b16 a[186:187], v122 offset:12288\n"
    "ds_read_b64_tr_b16 a[188:189], v123 offset:8192\n"
    "v_mfma_f32_16x16x32_bf16 v[248:251], a[152:155], v[88:91], v[248:251]\n"
    "v_exp_f32 v47, v47\n"
    "v_mfma_f32_16x16x32_bf16 v[252:255], a[156:159], v[88:91], v[252:255]\n"
    "ds_read_b64_tr_b16 a[190:191], v123 offset:12288\n"
    "v_cvt_pk_bf16_f32 v79, v46, v47\n"
    "; -- B7\n"
    "v_mfma_f32_16x16x32_bf16 v[40:43], a[64:67], a[16:19], v[100:103]\n"
    "v_exp_f32 v56, v56\n"
    "v_mfma_f32_16x16x32_bf16 v[40:43], a[68:71], a[20:23], v[40:43]\n"
    "ds_read_b128 a[96:99], v112 offset:24576\n"
    "ds_read_b128 a[100:103], v113 offset:24576\n"
    "s_waitcnt lgkmcnt(8)\n"
    "v_mfma_f32_16x16x32_bf16 v[40:43], a[72:75], a[24:27], v[40:43]\n"
    "v_exp_f32 v57, v57\n"
    "v_mfma_f32_16x16x32_bf16 v[40:43], a[76:79], a[28:31], v[40:43]\n"
    "ds_read_b128 a[104:107], v114 offset:24576\n"
    "v_cvt_pk_bf16_f32 v92, v56, v57\n"
    "v_mfma_f32_16x16x32_bf16 v[44:47], a[80:83], a[16:19], v[100:103]\n"
    "v_exp_f32 v58, v58\n"
    "v_mfma_f32_16x16x32_bf16 v[44:47], a[84:87], a[20:23], v[44:47]\n"
    "ds_read_b128 a[108:111], v115 offset:24576\n"
    "ds_read_b128 a[112:115], v112 offset:28672\n"
    "v_mfma_f32_16x16x32_bf16 v[44:47], a[88:91], a[24:27], v[44:47]\n"
    "v_exp_f32 v59, v59\n"
    "v_mfma_f32_16x16x32_bf16 v[44:47], a[92:95], a[28:31], v[44:47]\n"
    "ds_read_b128 a[116:119], v113 offset:28672\n"
    "v_cvt_pk_bf16_f32 v93, v58, v59\n"
    "v_mfma_f32_16x16x32_bf16 v[160:163], a[160:163], v[76:79], v[160:163]\n"
    "v_exp_f32 v60, v60\n"
    "v_mfma_f32_16x16x32_bf16 v[164:167], a[164:167], v[76:79], v[164:167]\n"
    "ds_read_b128 a[120:123], v114 offset:28672\n"
    "ds_read_b128 a[124:127], v115 offset:28672\n"
    "v_mfma_f32_16x16x32_bf16 v[168:171], a[168:171], v[76:79], v[168:171]\n"
    "v_exp_f32 v61, v61\n"
    "v_mfma_f32_16x16x32_bf16 v[172:175], a[172:175], v[76:79], v[172:175]\n"
    "ds_read_b64_tr_b16 a[128:129], v116 offset:16384\n"
    "v_cvt_pk_bf16_f32 v94, v60, v61\n"
    "v_mfma_f32_16x16x32_bf16 v[176:179], a[176:179], v[76:79], v[176:179]\n"
    "v_exp_f32 v62, v62\n"
    "s_waitcnt lgkmcnt(7)\n"
    "v_mfma_f32_16x16x32_bf16 v[180:183], a[180:183], v[76:79], v[180:183]\n"
    "ds_read_b64_tr_b16 a[130:131], v116 offset:20480\n"
    "ds_read_b64_tr_b16 a[132:133], v117 offset:16384\n"
    "v_mfma_f32_16x16x32_bf16 v[184:187], a[184:187], v[76:79], v[184:187]\n"
    "v_exp_f32 v63, v63\n"
    "v_mfma_f32_16x16x32_bf16 v[188:191], a[188:191], v[76:79], v[188:191]\n"
    "ds_read_b64_tr_b16 a[134:135], v117 offset:20480\n"
    "v_cvt_pk_bf16_f32 v95, v62, v63\n"
    "v_smfmac_f32_16x16x64_bf16 a[196:199], a[208:211], v[72:79], v126\n"
    "s_mov_b64 exec, 0xff\nbuffer_load_dwordx4 v[96:99], v22, %[srdw], %[cbase] offen sc1\ns_mov_b64 exec, -1\n"
    "; -- B8\n"
    "v_mfma_f32_16x16x32_bf16 v[56:59], a[64:67], a[48:51], v[108:111]\n"
    "v_exp_f32 v40, v40\n"
    "v_mfma_f32_16x16x32_bf16 v[56:59], a[68:71], a[52:55], v[56:59]\n"
    "ds_read_b64_tr_b16 a[136:137], v118 offset:16384\n"
    "ds_read_b64_tr_b16 a[138:139], v118 offset:20480\n"
    "v_mfma_f32_16x16x32_bf16 v[56:59], a[72:75], a[56:59], v[56:59]\n"
    "v_exp_f32 v41, v41\n"
    "v_mfma_f32_16x16x32_bf16 v[56:59], a[76:79], a[60:63], v[56:59]\n"
    "ds_read_b64_tr_b16 a[140:141], v119 offset:16384\n"
    "v_cvt_pk_bf16_f32 v72, v40, v41\n"
    "v_mfma_f32_16x16x32_bf16 v[60:63], a[80:83], a[48:51], v[108:111]\n"
    "v_exp_f32 v42, v42\n"
    "v_mfma_f32_16x16x32_bf16 v[60:63], a[84:87], a[52:55], v[60:63]\n"
    "ds_read_b64_tr_b16 a[142:143], v119 offset:20480\n"
    "ds_read_b64_tr_b16 a[144:145], v120 offset:16384\n"
    "v_mfma_f32_16x16x32_bf16 v[60:63], a[88:91], a[56:59], v[60:63]\n"
    "v_exp_f32 v43, v43\n"
    "v_mfma_f32_16x16x32_bf16 v[60:63], a[92:95], a[60:63], v[60:63]\n"
    "ds_read_b64_tr_b16 a[146:147], v120 offset:20480\n"
    "v_cvt_pk_bf16_f32 v73, v42, v43\n"
    "v_mfma_f32_16x16x32_bf16 v[224:227], a[160:163], v[92:95], v[224:227]\n"
    "v_exp_f32 v44, v44\n"
    "v_mfma_f32_16x16x32_bf16 v[228:231], a[164:167], v[92:95], v[228:231]\n"
    "ds_read_b64_tr_b16 a[148:149], v121 offset:16384\n"
    "ds_read_b64_tr_b16 a[150:151], v121 offset:20480\n"
    "v_mfma_f32_16x16x32_bf16 v[232:235], a[168:171], v[92:95], v[232:235]\n"
    "v_exp_f32 v45, v45\n"
    "v_mfma_f32_16x16x32_bf16 v[236:239], a[172:175], v[92:95], v[236:239]\n"
    "ds_read_b64_tr_b16 a[152:153], v122 offset:16384\n"
    "v_cvt_pk_bf16_f32 v74, v44, v45\n"
    "v_mfma_f32_16x16x32_bf16 v[240:243], a[176:179], v[92:95], v[240:243]\n"
    "v_exp_f32 v46, v46\n"
    "v_mfma_f32_16x16x32_bf16 v[244:247], a[180:183], v[92:95], v[244:247]\n"
    "ds_read_b64_tr_b16 a[154:155], v122 offset:20480\n"
    "ds_read_b64_tr_b16 a[156:157], v123 offset:16384\n"
    "v_mfma_f32_16x16x32_bf16 v[248:251], a[184:187], v[92:95], v[248:251]\n"
    "v_exp_f32 v47, v47\n"
    "v_mfma_f32_16x16x32_bf16 v[252:255], a[188:191], v[92:95], v[252:255]\n"
    "ds_read_b64_tr_b16 a[158:159], v123 offset:20480\n"
    "v_cvt_pk_bf16_f32 v75, v46, v47\n"
    "v_smfmac_f32_16x16x64_bf16 a[204:207], a[208:211], v[88:95], v126\n"
    "; -- B9\n"
    "v_mfma_f32_16x16x32_bf16 v[40:43], a[96:99], a[16:19], v[100:103]\n"
    "v_exp_f32 v56, v56\n"
    "v_mfma_f32_16x16x32_bf16 v[40:43], a[100:103], a[20:23], v[40:43]\n"
    "ds_read_b128 a[64:67], v112 offset:32768\n"
    "ds_read_b128 a[68:71], v113 offset:32768\n"
    "s_waitcnt lgkmcnt(8)\n"
    "v_mfma_f32_16x16x32_bf16 v[40:43], a[104:107], a[24:27], v[40:43]\n"
    "v_exp_f32 v57, v57\n"
    "v_mfma_f32_16x16x32_bf16 v[40:43], a[108:111], a[28:31], v[40:43]\n"
    "ds_read_b128 a[72:75], v114 offset:32768\n"
    "v_cvt_pk_bf16_f32 v88, v56, v57\n"
    "v_mfma_f32_16x16x32_bf16 v[44:47], a[112:115], a[16:19], v[100:103]\n"
    "v_exp_f32 v58, v58\n"
    "v_mfma_f32_16x16x32_bf16 v[44:47], a[116:119], a[20:23], v[44:47]\n"
    "ds_read_b128 a[76:79], v115 offset:32768\n"
    "ds_read_b128 a[80:83], v112 offset:36864\n"
    "v_mfma_f32_16x16x32_bf16 v[44:47], a[120:123], a[24:27], v[44:47]\n"
    "v_exp_f32 v59, v59\n"
    "v_mfma_f32_16x16x32_bf16 v[44:47], a[124:127], a[28:31], v[44:47]\n"
    "ds_read_b128 a[84:87], v113 offset:36864\n"
    "v_cvt_pk_bf16_f32 v89, v58, v59\n"
    "v_mfma_f32_16x16x32_bf16 v[160:163], a[128:131], v[72:75], v[160:163]\n"
    "v_exp_f32 v60, v60\n"
    "v_mfma_f32_16x16x32_bf16 v[164:167], a[132:135], v[72:75], v[164:167]\n"
    "ds_read_b128 a[88:91], v114 offset:36864\n"
    "ds_read_b128 a[92:95], v115 offset:36864\n"
    "v_mfma_f32_16x16x32_bf16 v[168:171], a[136:139], v[72:75], v[168:171]\n"
    "v_exp_f32 v61, v61\n"
    "v_mfma_f32_16x16x32_bf16 v[172:175], a[140:143], v[72:75], v[172:175]\n"
    "ds_read_b64_tr_b16 a[160:161], v116 offset:24576\n"
    "v_cvt_pk_bf16_f32 v90, v60, v61\n"
    "v_mfma_f32_16x16x32_bf16 v[176:179], a[144:147], v[72:75], v[176:179]\n"
    "v_exp_f32 v62, v62\n"
    "s_waitcnt lgkmcnt(7)\n"
    "v_mfma_f32_16x16x32_bf16 v[180:183], a[148:151], v[72:75], v[180:183]\n"
    "ds_read_b64_tr_b16 a[162:163], v116 offset:28672\n"
    "ds_read_b64_tr_b16 a[164:165], v117 offset:24576\n"
    "v_mfma_f32_16x16x32_bf16 v[184:187], a[152:155], v[72:75], v[184:187]\n"
    "v_exp_f32 v63, v63\n"
    "v_mfma_f32_16x16x32_bf16 v[188:191], a[156:159], v[72:75], v[188:191]\n"
    "ds_read_b64_tr_b16 a[166:167], v117 offset:28672\n"
    "v_cvt_pk_bf16_f32 v91, v62, v63\n"
    "; -- B10\n"
    "v_mfma_f32_16x16x32_bf16 v[56:59], a[96:99], a[48:51], v[108:111]\n"
    "v_exp_f32 v40, v40\n"
    "v_mfma_f32_16x16x32_bf16 v[56:59], a[100:103], a[52:55], v[56:59]\n"
    "ds_read_b64_tr_b16 a[168:169], v118 offset:24576\n"
    "ds_read_b64_tr_b16 a[170:171], v118 offset:28672\n"
    "v_mfma_f32_16x16x32_bf16 v[56:59], a[104:107], a[56:59], v[56:59]\n"
    "v_exp_f32 v41, v41\n"
    "v_mfma_f32_16x16x32_bf16 v[56:59], a[108:111], a[60:63], v[56:59]\n"
    "ds_read_b64_tr_b16 a[172:173], v119 offset:24576\n"
    "v_cvt_pk_bf16_f32 v76, v40, v41\n"
    "v_mfma_f32_16x16x32_bf16 v[60:63], a[112:115], a[48:51], v[108:111]\n"
    "v_exp_f32 v42, v42\n"
    "v_mfma_f32_16x16x32_bf16 v[60:63], a[116:119], a[52:55], v[60:63]\n"
    "ds_read_b64_tr_b16 a[174:175], v119 offset:28672\n"
    "ds_read_b64_tr_b16 a[176:177], v120 offset:24576\n"
    "v_mfma_f32_16x16x32_bf16 v[60:63], a[120:123], a[56:59], v[60:63]\n"
    "v_exp_f32 v43, v43\n"
    "v_mfma_f32_16x16x32_bf16 v[60:63], a[124:127], a[60:63], v[60:63]\n"
    "ds_read_b64_tr_b16 a[178:179], v120 offset:28672\n"
    "v_cvt_pk_bf16_f32 v77, v42, v43\n"
    "v_mfma_f32_16x16x32_bf16 v[224:227], a[128:131], v[88:91], v[224:227]\n"
    "v_exp_f32 v44, v44\n"
    "v_mfma_f32_16x16x32_bf16 v[228:231], a[132:135], v[88:91], v[228:231]\n"
    "ds_read_b64_tr_b16 a[180:181], v121 offset:24576\n"
    "ds_read_b64_tr_b16 a[182:183], v121 offset:28672\n"
    "v_mfma_f32_16x16x32_bf16 v[232:235], a[136:139], v[88:91], v[232:235]\n"
    "v_exp_f32 v45, v45\n"
    "v_mfma_f32_16x16x32_bf16 v[236:239], a[140:143], v[88:91], v[236:239]\n"
    "ds_read_b64_tr_b16 a[184:185], v122 offset:24576\n"
    "v_cvt_pk_bf16_f32 v78, v44, v45\n"
    "v_mfma_f32_16x16x32_bf16 v[240:243], a[144:147], v[88:91], v[240:243]\n"
    "v_exp_f32 v46, v46\n"
    "v_mfma_f32_16x16x32_bf16 v[244:247], a[148:151], v[88:91], v[244:247]\n"
    "ds_read_b64_tr_b16 a[186:187], v122 offset:28672\n"
    "ds_read_b64_tr_b16 a[188:189], v123 offset:24576\n"
    "v_mfma_f32_16x16x32_bf16 v[248:251], a[152:155], v[88:91], v[248:251]\n"
    "v_exp_f32 v47, v47\n"
    "v_mfma_f32_16x16x32_bf16 v[252:255], a[156:159], v[88:91], v[252:255]\n"
    "ds_read_b64_tr_b16 a[190:191], v123 offset:28672\n"
    "v_cvt_pk_bf16_f32 v79, v46, v47\n"
    "s_waitcnt vmcnt(0)\n"
    "v_cmp_eq_u64 vcc, v[96:97], v[26:27]\n"
    "s_nop 0\n"
    "s_or_b64 vcc, vcc, s[88:89]\ns_cmp_eq_u64 vcc, -1\ns_cselect_b32 s80, 1, 0\ns_cselect_b32 s83, 0, 0x40000000\n"
    "v_or_b32 v104, s83, v24\n"
    "v_or_b32 v105, s83, v23\n"
    "buffer_load_dwordx2 a[242:243], v104, %[srdw], s56 offen sc1\n"
    "buffer_load_dwordx2 a[244:245], v104, %[srdw], s57 offen sc1\n"
    "buffer_load_dwordx2 a[246:247], v104, %[srdw], s58 offen sc1\n"
    "buffer_load_dwordx2 a[248:249], v104, %[srdw], s59 offen sc1\n"
    "buffer_load_dwordx2 a[250:251], v104, %[srdw], s60 offen sc1\n"
    "buffer_load_dwordx2 a[252:253], v104, %[srdw], s61 offen sc1\n"
    "buffer_load_dwordx2 a[254:255], v104, %[srdw], s62 offen sc1\n"
    "; -- B11\n"
    "v_mfma_f32_16x16x32_bf16 v[40:43], a[64:67], a[16:19], v[100:103]\n"
    "v_exp_f32 v56, v56\n"
    "v_mfma_f32_16x16x32_bf16 v[40:43], a[68:71], a[20:23], v[40:43]\n"
    "ds_read_b128 a[96:99], v112 offset:40960\n"
    "ds_read_b128 a[100:103], v113 offset:40960\n"
    "s_waitcnt lgkmcnt(3)\n"
    "v_mfma_f32_16x16x32_bf16 v[40:43], a[72:75], a[24:27], v[40:43]\n"
    "v_exp_f32 v57, v57\n"
    "v_mfma_f32_16x16x32_bf16 v[40:43], a[76:79], a[28:31], v[40:43]\n"
    "ds_read_b128 a[104:107], v114 offset:40960\n"
    "v_cvt_pk_bf16_f32 v92, v56, v57\n"
    "v_mfma_f32_16x16x32_bf16 v[44:47], a[80:83], a[16:19], v[100:103]\n"
    "v_exp_f32 v58, v58\n"
    "v_mfma_f32_16x16x32_bf16 v[44:47], a[84:87], a[20:23], v[44:47]\n"
    "ds_read_b128 a[108:111], v115 offset:40960\n"
    "ds_read_b128 a[112:115], v112 offset:45056\n"
    "v_mfma_f32_16x16x32_bf16 v[44:47], a[88:91], a[24:27], v[44:47]\n"
    "v_exp_f32 v59, v59\n"
    "v_mfma_f32_16x16x32_bf16 v[44:47], a[92:95], a[28:31], v[44:47]\n"
    "ds_read_b128 a[116:119], v113 offset:45056\n"
    "v_cvt_pk_bf16_f32 v93, v58, v59\n"
    "v_mfma_f32_16x16x32_bf16 v[160:163], a[160:163], v[76:79], v[160:163]\n"
    "v_exp_f32 v60, v60\n"
    "v_mfma_f32_16x16x32_bf16 v[164:167], a[164:167], v[76:79], v[164:167]\n"
    "ds_read_b128 a[120:123], v114 offset:45056\n"
    "ds_read_b128 a[124:127], v115 offset:45056\n"
    "v_mfma_f32_16x16x32_bf16 v[168:171], a[168:171], v[76:79], v[168:171]\n"
    "v_exp_f32 v61, v61\n"
    "v_mfma_f32_16x16x32_bf16 v[172:175], a[172:175], v[76:79], v[172:175]\n"
    "ds_read_b64_tr_b16 a[128:129], v116 offset:32768\n"
    "v_cvt_pk_bf16_f32 v94, v60, v61\n"
    "v_mfma_f32_16x16x32_bf16 v[176:179], a[176:179], v[76:79], v[176:179]\n"
    "v_exp_f32 v62, v62\n"
    "v_mfma_f32_16x16x32_bf16 v[180:183], a[180:183], v[76:79], v[180:183]\n"
    "ds_read_b64_tr_b16 a[130:131], v116 offset:36864\n"
    "ds_read_b64_tr_b16 a[132:133], v117 offset:32768\n"
    "v_mfma_f32_16x16x32_bf16 v[184:187], a[184:187], v[76:79], v[184:187]\n"
    "v_exp_f32 v63, v63\n"
    "s_waitcnt lgkmcnt(8)\n"
    "v_mfma_f32_16x16x32_bf16 v[188:191], a[188:191], v[76:79], v[188:191]\n"
    "ds_read_b64_tr_b16 a[134:135], v117 offset:36864\n"
    "v_cvt_pk_bf16_f32 v95, v62, v63\n"
    "v_smfmac_f32_16x16x64_bf16 a[196:199], a[208:211], v[72:79], v126\n"
    "buffer_load_dwordx4 a[214:217], v105, %[srdw], s56 offen sc1\n"
    "buffer_load_dwordx4 a[218:221], v105, %[srdw], s57 offen sc1\n"
    "buffer_load_dwordx4 a[222:225], v105, %[srdw], s58 offen sc1\n"
    "buffer_load_dwordx4 a[226:229], v105, %[srdw], s59 offen sc1\n"
    "buffer_load_dwordx4 a[230:233], v105, %[srdw], s60 offen sc1\n"
    "buffer_load_dwordx4 a[234:237], v105, %[srdw], s61 offen sc1\n"
    "buffer_load_dwordx4 a[238:241], v105, %[srdw], s62 offen sc1\n"
    "; -- B12\n"
    "v_mfma_f32_16x16x32_bf16 v[56:59], a[64:67], a[48:51], v[108:111]\n"
    "v_exp_f32 v40, v40\n"
    "v_mfma_f32_16x16x32_bf16 v[56:59], a[68:71], a[52:55], v[56:59]\n"
    "ds_read_b64_tr_b16 a[136:137], v118 offset:32768\n"
    "ds_read_b64_tr_b16 a[138:139], v118 offset:36864\n"
    "v_mfma_f32_16x16x32_bf16 v[56:59], a[72:75], a[56:59], v[56:59]\n"
    "v_exp_f32 v41, v41\n"
    "v_mfma_f32_16x16x32_bf16 v[56:59], a[76:79], a[60:63], v[56:59]\n"
    "ds_read_b64_tr_b16 a[140:141], v119 offset:32768\n"
    "v_cvt_pk_bf16_f32 v72, v40, v41\n"
    "v_mfma_f32_16x16x32_bf16 v[60:63], a[80:83], a[48:51], v[108:111]\n"
    "v_exp_f32 v42, v42\n"
    "v_mfma_f32_16x16x32_bf16 v[60:63], a[84:87], a[52:55], v[60:63]\n"
    "ds_read_b64_tr_b16 a[142:143], v119 offset:36864\n"
    "ds_read_b64_tr_b16 a[144:145], v120 offset:32768\n"
    "v_mfma_f32_16x16x32_bf16 v[60:63], a[88:91], a[56:59], v[60:63]\n"
    "v_exp_f32 v43, v43\n"
    "v_mfma_f32_16x16x32_bf16 v[60:63], a[92:95], a[60:63], v[60:63]\n"
    "ds_read_b64_tr_b16 a[146:147], v120 offset:36864\n"
    "v_cvt_pk_bf16_f32 v73, v42, v43\n"
    "v_mfma_f32_16x16x32_bf16 v[224:227], a[160:163], v[92:95], v[224:227]\n"
    "v_exp_f32 v44, v44\n"
    "v_mfma_f32_16x16x32_bf16 v[228:231], a[164:167], v[92:95], v[228:231]\n"
    "ds_read_b64_tr_b16 a[148:149], v121 offset:32768\n"
    "ds_read_b64_tr_b16 a[150:151], v121 offset:36864\n"
    "v_mfma_f32_16x16x32_bf16 v[232:235], a[168:171], v[92:95], v[232:235]\n"
    "v_exp_f32 v45, v45\n"
    "v_mfma_f32_16x16x32_bf16 v[236:239], a[172:175], v[92:95], v[236:239]\n"
    "ds_read_b64_tr_b16 a[152:153], v122 offset:32768\n"
    "v_cvt_pk_bf16_f32 v74, v44, v45\n"
    "v_mfma_f32_16x16x32_bf16 v[240:243], a[176:179], v[92:95], v[240:243]\n"
    "v_exp_f32 v46, v46\n"
    "v_mfma_f32_16x16x32_bf16 v[244:247], a[180:183], v[92:95], v[244:247]\n"
    "ds_read_b64_tr_b16 a[154:155], v122 offset:36864\n"
    "ds_read_b64_tr_b16 a[156:157], v123 offset:32768\n"
    "v_mfma_f32_16x16x32_bf16 v[248:251], a[184:187], v[92:95], v[248:251]\n"
    "v_exp_f32 v47, v47\n"
    "v_mfma_f32_16x16x32_bf16 v[252:255], a[188:191], v[92:95], v[252:255]\n"
    "ds_read_b64_tr_b16 a[158:159], v123 offset:36864\n"
    "v_cvt_pk_bf16_f32 v75, v46, v47\n"
    "v_smfmac_f32_16x16x64_bf16 a[204:207], a[208:211], v[88:95], v126\n"
    "; -- B13\n"
    "v_mfma_f32_16x16x32_bf16 v[40:43], a[96:99], a[16:19], v[100:103]\n"
    "v_exp_f32 v56, v56\n"
    "v_mfma_f32_16x16x32_bf16 v[40:43], a[100:103], a[20:23], v[40:43]\n"
    "ds_read_b64_tr_b16 a[160:161], v116 offset:40960\n"
    "ds_read_b64_tr_b16 a[162:163], v116 offset:45056\n"
    "v_mfma_f32_16x16x32_bf16 v[40:43], a[104:107], a[24:27], v[40:43]\n"
    "v_exp_f32 v57, v57\n"
    "s_waitcnt lgkmcnt(6)\n"
    "v_mfma_f32_16x16x32_bf16 v[40:43], a[108:111], a[28:31], v[40:43]\n"
    "ds_read_b64_tr_b16 a[164:165], v117 offset:40960\n"
    "v_cvt_pk_bf16_f32 v88, v56, v57\n"
    "v_mfma_f32_16x16x32_bf16 v[44:47], a[112:115], a[16:19], v[100:103]\n"
    "v_exp_f32 v58, v58\n"
    "v_mfma_f32_16x16x32_bf16 v[44:47], a[116:119], a[20:23], v[44:47]\n"
    "ds_read_b64_tr_b16 a[166:167], v117 offset:45056\n"
    "ds_read_b64_tr_b16 a[168:169], v118 offset:40960\n"
    "v_mfma_f32_16x16x32_bf16 v[44:47], a[120:123], a[24:27], v[44:47]\n"
    "v_exp_f32 v59, v59\n"
    "v_mfma_f32_16x16x32_bf16 v[44:47], a[124:127], a[28:31], v[44:47]\n"
    "ds_read_b64_tr_b16 a[170:171], v118 offset:45056\n"
    "v_cvt_pk_bf16_f32 v89, v58, v59\n"
    "v_mfma_f32_16x16x32_bf16 v[160:163], a[128:131], v[72:75], v[160:163]\n"
    "v_exp_f32 v60, v60\n"
    "v_mfma_f32_16x16x32_bf16 v[164:167], a[132:135], v[72:75], v[164:167]\n"
    "ds_read_b64_tr_b16 a[172:173], v119 offset:40960\n"
    "ds_read_b64_tr_b16 a[174:175], v119 offset:45056\n"
    "v_mfma_f32_16x16x32_bf16 v[168:171], a[136:139], v[72:75], v[168:171]\n"
    "v_exp_f32 v61, v61\n"
    "v_mfma_f32_16x16x32_bf16 v[172:175], a[140:143], v[72:75], v[172:175]\n"
    "ds_read_b64_tr_b16 a[176:177], v120 offset:40960\n"
    "v_cvt_pk_bf16_f32 v90, v60, v61\n"
    "v_mfma_f32_16x16x32_bf16 v[176:179], a[144:147], v[72:75], v[176:179]\n"
    "v_exp_f32 v62, v62\n"
    "v_mfma_f32_16x16x32_bf16 v[180:183], a[148:151], v[72:75], v[180:183]\n"
    "ds_read_b64_tr_b16 a[178:179], v120 offset:45056\n"
    "ds_read_b64_tr_b16 a[180:181], v121 offset:40960\n"
    "s_waitcnt lgkmcnt(8)\n"
    "v_mfma_f32_16x16x32_bf16 v[184:187], a[152:155], v[72:75], v[184:187]\n"
    "v_exp_f32 v63, v63\n"
    "v_mfma_f32_16x16x32_bf16 v[188:191], a[156:159], v[72:75], v[188:191]\n"
    "ds_read_b64_tr_b16 a[182:183], v121 offset:45056\n"
    "v_cvt_pk_bf16_f32 v91, v62, v63\n"
    "; -- B14\n"
    "v_mfma_f32_16x16x32_bf16 v[56:59], a[96:99], a[48:51], v[108:111]\n"
    "v_exp_f32 v40, v40\n"
    "v_mfma_f32_16x16x32_bf16 v[56:59], a[100:103], a[52:55], v[56:59]\n"
    "ds_read_b64_tr_b16 a[184:185], v122 offset:40960\n"
    "ds_read_b64_tr_b16 a[186:187], v122 offset:45056\n"
    "v_mfma_f32_16x16x32_bf16 v[56:59], a[104:107], a[56:59], v[56:59]\n"
    "v_exp_f32 v41, v41\n"
    "v_mfma_f32_16x16x32_bf16 v[56:59], a[108:111], a[60:63], v[56:59]\n"
    "ds_read_b64_tr_b16 a[188:189], v123 offset:40960\n"
    "v_cvt_pk_bf16_f32 v76, v40, v41\n"
    "v_mfma_f32_16x16x32_bf16 v[60:63], a[112:115], a[48:51], v[108:111]\n"
    "v_exp_f32 v42, v42\n"
    "v_mfma_f32_16x16x32_bf16 v[60:63], a[116:119], a[52:55], v[60:63]\n"
    "ds_read_b64_tr_b16 a[190:191], v123 offset:45056\n"
    "v_mfma_f32_16x16x32_bf16 v[60:63], a[120:123], a[56:59], v[60:63]\n"
    "v_exp_f32 v43, v43\n"
    "v_mfma_f32_16x16x32_bf16 v[60:63], a[124:127], a[60:63], v[60:63]\n"
    "v_cvt_pk_bf16_f32 v77, v42, v43\n"
    "v_mfma_f32_16x16x32_bf16 v[224:227], a[128:131], v[88:91], v[224:227]\n"
    "v_exp_f32 v44, v44\n"
    "v_mfma_f32_16x16x32_bf16 v[228:231], a[132:135], v[88:91], v[228:231]\n"
    "v_mfma_f32_16x16x32_bf16 v[232:235], a[136:139], v[88:91], v[232:235]\n"
    "v_exp_f32 v45, v45\n"
    "v_mfma_f32_16x16x32_bf16 v[236:239], a[140:143], v[88:91], v[236:239]\n"
    "v_cvt_pk_bf16_f32 v78, v44, v45\n"
    "v_mfma_f32_16x16x32_bf16 v[240:243], a[144:147], v[88:91], v[240:243]\n"
    "v_exp_f32 v46, v46\n"
    "v_mfma_f32_16x16x32_bf16 v[244:247], a[148:151], v[88:91], v[244:247]\n"
    "v_mfma_f32_16x16x32_bf16 v[248:251], a[152:155], v[88:91], v[248:251]\n"
    "v_exp_f32 v47, v47\n"
    "v_mfma_f32_16x16x32_bf16 v[252:255], a[156:159], v[88:91], v[252:255]\n"
    "v_cvt_pk_bf16_f32 v79, v46, v47\n"
    "; -- F1\n"
    "s_nop 1\n"
    "v_mfma_f32_16x16x32_bf16 v[160:163], a[160:163], v[76:79], v[160:163]\n"
    "v_exp_f32 v56, v56\n"
    "s_waitcnt lgkmcnt(1)\n"
    "v_mfma_f32_16x16x32_bf16 v[164:167], a[164:167], v[76:79], v[164:167]\n"
    "v_exp_f32 v57, v57\n"
    "v_mfma_f32_16x16x32_bf16 v[168:171], a[168:171], v[76:79], v[168:171]\n"
    "v_exp_f32 v58, v58\n"
    "v_mfma_f32_16x16x32_bf16 v[172:175], a[172:175], v[76:79], v[172:175]\n"
    "v_exp_f32 v59, v59\n"
    "v_mfma_f32_16x16x32_bf16 v[176:179], a[176:179], v[76:79], v[176:179]\n"
    "v_exp_f32 v60, v60\n"
    "v_mfma_f32_16x16x32_bf16 v[180:183], a[180:183], v[76:79], v[180:183]\n"
    "v_exp_f32 v61, v61\n"
    "v_mfma_f32_16x16x32_bf16 v[184:187], a[184:187], v[76:79], v[184:187]\n"
    "v_exp_f32 v62, v62\n"
    "s_waitcnt lgkmcnt(0)\n"
    "v_mfma_f32_16x16x32_bf16 v[188:191], a[188:191], v[76:79], v[188:191]\n"
    "v_exp_f32 v63, v63\n"
    "v_smfmac_f32_16x16x64_bf16 a[196:199], a[208:211], v[72:79], v126\n"
    "v_cvt_pk_bf16_f32 v92, v56, v57\n"
    "v_cvt_pk_bf16_f32 v93, v58, v59\n"
    "v_cvt_pk_bf16_f32 v94, v60, v61\n"
    "v_cvt_pk_bf16_f32 v95, v62, v63\n"
    "; -- F3\n"
    "s_nop 1\n"
    "v_smfmac_f32_16x16x64_bf16 a[204:207], a[208:211], v[88:95], v126\n"
    "s_add_u32 s82, %[pb0], 4096\n"
    "v_cvt_pk_bf16_f32 v48, v160, v161\n"
    "v_mfma_f32_16x16x32_bf16 v[224:227], a[160:163], v[92:95], v[224:227]\n"
    "v_cvt_pk_bf16_f32 v49, v162, v163\n"
    "v_cvt_pk_bf16_f32 v50, v164, v165\n"
    "v_mfma_f32_16x16x32_bf16 v[228:231], a[164:167], v[92:95], v[228:231]\n"
    "v_cvt_pk_bf16_f32 v51, v166, v167\n"
    "v_mfma_f32_16x16x32_bf16 v[232:235], a[168:171], v[92:95], v[232:235]\n"
    "buffer_store_dwordx4 v[48:51], v127, %[srdw], s82 offen sc1\n"
    "v_mfma_f32_16x16x32_bf16 v[236:239], a[172:175], v[92:95], v[236:239]\n"
    "v_cvt_pk_bf16_f32 v52, v168, v169\n"
    "v_cvt_pk_bf16_f32 v53, v170, v171\n"
    "v_mfma_f32_16x16x32_bf16 v[240:243], a[176:179], v[92:95], v[240:243]\n"
    "v_cvt_pk_bf16_f32 v54, v172, v173\n"
    "v_cvt_pk_bf16_f32 v55, v174, v175\n"
    "v_mfma_f32_16x16x32_bf16 v[244:247], a[180:183], v[92:95], v[244:247]\n"
    "buffer_store_dwordx4 v[52:55], v127, %[srdw], s82 offen offset:1024 sc1\n"
    "v_mfma_f32_16x16x32_bf16 v[248:251], a[184:187], v[92:95], v[248:251]\n"
    "v_cvt_pk_bf16_f32 v80, v176, v177\n"
    "v_cvt_pk_bf16_f32 v81, v178, v179\n"
    "v_mfma_f32_16x16x32_bf16 v[252:255], a[188:191], v[92:95], v[252:255]\n"
    "v_cvt_pk_bf16_f32 v82, v180, v181\n"
    "v_cvt_pk_bf16_f32 v83, v182, v183\n"
    "buffer_store_dwordx4 v[80:83], v127, %[srdw], s82 offen offset:2048 sc1\n"
    "v_cvt_pk_bf16_f32 v84, v184, v185\n"
    "v_cvt_pk_bf16_f32 v85, v186, v187\n"
    "v_cvt_pk_bf16_f32 v86, v188, v189\n"
    "v_cvt_pk_bf16_f32 v87, v190, v191\n"
    "buffer_store_dwordx4 v[84:87], v127, %[srdw], s82 offen offset:3072 sc1\n"
    "v_accvgpr_read_b32 v28, a196\n"
    "v_accvgpr_read_b32 v29, a197\n"
    "v_accvgpr_read_b32 v30, a204\n"
    "v_accvgpr_read_b32 v31, a205\n"
    "v_add_f32 v28, v28, v29\n"
    "v_add_f32 v30, v30, v31\n"
    "v_cndmask_b32 v29, v28, v30, s[86:87]\n"
    "v_cndmask_b32 v28, %[m1], %[m3], s[86:87]\n"
    "v_cmp_nlt_f32 vcc, v29, v20\n"
    "s_mov_b32 exec_hi, 0\nbuffer_store_dwordx2 v[28:29], v21, %[srdw], %[sbase] offen offset:128 sc1\ns_mov_b32 exec_hi, -1\n"
    "ds_write_b64 v19, v[28:29] offset:512\n"
    "s_cmp_lg_u64 vcc, 0\n"
    "s_cselect_b32 s83, 2, 0\n"
    "s_or_b32 %[bad], %[bad], s83\n"
    "s_add_u32 s82, %[pb1], 4096\n"
    "v_cvt_pk_bf16_f32 v32, v224, v225\n"
    "v_cvt_pk_bf16_f32 v33, v226, v227\n"
    "v_cvt_pk_bf16_f32 v34, v228, v229\n"
    "v_cvt_pk_bf16_f32 v35, v230, v231\n"
    "buffer_store_dwordx4 v[32:35], v127, %[srdw], s82 offen sc1\n"
    "v_cvt_pk_bf16_f32 v36, v232, v233\n"
    "v_cvt_pk_bf16_f32 v37, v234, v235\n"
    "v_cvt_pk_bf16_f32 v38, v236, v237\n"
    "v_cvt_pk_bf16_f32 v39, v238, v239\n"
    "buffer_store_dwordx4 v[36:39], v127, %[srdw], s82 offen offset:1024 sc1\n"
    "v_cvt_pk_bf16_f32 v48, v240, v241\n"
    "v_cvt_pk_bf16_f32 v49, v242, v243\n"
    "v_cvt_pk_bf16_f32 v50, v244, v245\n"
    "v_cvt_pk_bf16_f32 v51, v246, v247\n"
    "buffer_store_dwordx4 v[48:51], v127, %[srdw], s82 offen offset:2048 sc1\n"
    "v_cvt_pk_bf16_f32 v52, v248, v249\n"
    "v_cvt_pk_bf16_f32 v53, v250, v251\n"
    "v_cvt_pk_bf16_f32 v54, v252, v253\n"
    "v_cvt_pk_bf16_f32 v55, v254, v255\n"
    "buffer_store_dwordx4 v[52:55], v127, %[srdw], s82 offen offset:3072 sc1\n"
    "v_mov_b32 v30, s91\n"
    "v_mov_b32 v31, %[bad]\n"
    "ds_write_b32 v30, v31\n"
    "v_add_u32 v126, 131072, v127\n"
    "s_cmp_eq_u32 %[own], 0\n"
    "s_cbranch_scc1 Lo0%=\n"
    "s_cmp_eq_u32 %[own], 2\n"
    "s_cbranch_scc1 Lo2%=\n"
    "ds_write_b128 v126, v[128:131] offset:0\n"
    "ds_write_b128 v126, v[132:135] offset:1024\n"
    "ds_write_b128 v126, v[136:139] offset:2048\n"
    "ds_write_b128 v126, v[140:143] offset:3072\n"
    "ds_write_b128 v126, v[144:147] offset:4096\n"
    "ds_write_b128 v126, v[148:151] offset:5120\n"
    "ds_write_b128 v126, v[152:155] offset:6144\n"
    "ds_write_b128 v126, v[156:159] offset:7168\n"
    "ds_write_b128 v126, v[160:163] offset:8192\n"
    "ds_write_b128 v126, v[164:167] offset:9216\n"
    "ds_write_b128 v126, v[168:171] offset:10240\n"
    "ds_write_b128 v126, v[172:175] offset:11264\n"
    "ds_write_b128 v126, v[176:179] offset:12288\n"
    "ds_write_b128 v126, v[180:183] offset:13312\n"
    "ds_write_b128 v126, v[184:187] offset:14336\n"
    "ds_write_b128 v126, v[188:191] offset:15360\n"
    "s_branch Lo0%=\n"
    "Lo2%=:\n"
    "Lh10_%=:\n"
    "s_cbranch_execz Lr10_%=\n"
    "ds_write_b128 v126, v[192:195] offset:0\n"
    "ds_write_b128 v126, v[196:199] offset:1024\n"
    "ds_write_b128 v126, v[200:203] offset:2048\n"
    "ds_write_b128 v126, v[204:207] offset:3072\n"
    "ds_write_b128 v126, v[208:211] offset:4096\n"
    "ds_write_b128 v126, v[212:215] offset:5120\n"
    "ds_write_b128 v126, v[216:219] offset:6144\n"
    "ds_write_b128 v126, v[220:223] offset:7168\n"
    "ds_write_b128 v126, v[224:227] offset:8192\n"
    "ds_write_b128 v126, v[228:231] offset:9216\n"
    "ds_write_b128 v126, v[232:235] offset:10240\n"
    "ds_write_b128 v126, v[236:239] offset:11264\n"
    "ds_write_b128 v126, v[240:243] offset:12288\n"
    "ds_write_b128 v126, v[244:247] offset:13312\n"
    "ds_write_b128 v126, v[248:251] offset:14336\n"
    "ds_write_b128 v126, v[252:255] offset:15360\n"
    "Lo0%=:\n"
    "Lh11_%=:\n"
    "s_cbranch_execz Lr11_%=\n"
    "v_add_u32 v126, %[m0w], v126\n"
    "v_mov_b32 v30, 163776\n"
    "v_add_u32 v19, s90, v24\n"
    "s_waitcnt vmcnt(0)\n"
    "s_and_b32 s83, %[bad], s78\n"
    "s_bitcmp1_b32 s83, 1\n"
    "s_cselect_b32 s83, 0x40000000, %[sbase]\n"
    "s_mov_b64 exec, 0x10001\nbuffer_store_dwordx2 v[26:27], v18, %[srdw], s83 offen offset:264 sc1\ns_mov_b64 exec, -1\n"
    "s_waitcnt lgkmcnt(0)\n"
    "s_barrier\n"
    "ds_read_b128 v[220:223], v30\n"
    "ds_read_b128 v[176:179], v126 offset:0\n"
    "ds_read_b128 v[180:183], v126 offset:1024\n"
    "ds_read_b128 v[184:187], v126 offset:8192\n"
    "ds_read_b128 v[188:191], v126 offset:9216\n"
    "ds_read_b64 v[78:79], v19 offset:0\n"
    "ds_read_b64 v[174:175], v19 offset:512\n"
    "s_waitcnt lgkmcnt(6)\n"
    "v_or3_b32 v220, v220, v221, v222\n"
    "v_or_b32 v220, v220, v223\n"
    "s_nop 0\n"
    "v_readfirstlane_b32 %[redo], v220\n"
    "s_waitcnt lgkmcnt(0)\n"
    "s_and_b32 %[redo], %[redo], s78\n"
    "s_cbranch_scc1 Lredo%=\n"
    "s_cmp_eq_u32 s80, 1\n"
    "s_cbranch_scc1 Lq2%=\n"
    "s_mov_b64 exec, 0xff\n"
    "buffer_load_dwordx4 v[96:99], v22, %[srdw], %[cbase] offen sc1\n"
    "s_mov_b64 exec, -1\n"
    "s_mov_b32 s79, 0x100000\n"
    "LsA%=:\n"
    "s_waitcnt vmcnt(0)\n"
    "v_cmp_eq_u64 vcc, v[96:97], v[26:27]\n"
    "s_nop 0\n"
    "s_or_b64 vcc, vcc, s[88:89]\n"
    "Lh12_%=:\n"
    "s_cbranch_execz Lr12_%=\n"
    "s_cmp_eq_u64 vcc, -1\n"
    "s_cbranch_scc1 LeA%=\n"
    "s_sub_u32 s79, s79, 1\n"
    "s_cbranch_scc1 LeA%=\n"
    "s_sleep 1\n"
    "s_mov_b64 exec, 0xff\n"
    "buffer_load_dwordx4 v[96:99], v22, %[srdw], %[cbase] offen sc1\n"
    "s_mov_b64 exec, -1\n"
    "s_branch LsA%=\n"
    "LeA%=:\n"
    "buffer_load_dwordx2 v[64:65], v24, %[srdw], s56 offen sc1\n"
    "buffer_load_dwordx2 v[66:67], v24, %[srdw], s57 offen sc1\n"
    "buffer_load_dwordx2 v[68:69], v24, %[srdw], s58 offen sc1\n"
    "buffer_load_dwordx2 v[70:71], v24, %[srdw], s59 offen sc1\n"
    "buffer_load_dwordx2 v[72:73], v24, %[srdw], s60 offen sc1\n"
    "buffer_load_dwordx2 v[74:75], v24, %[srdw], s61 offen sc1\n"
    "buffer_load_dwordx2 v[76:77], v24, %[srdw], s62 offen sc1\n"
    "buffer_load_dwordx4 v[32:35], v23, %[srdw], s56 offen sc1\n"
    "buffer_load_dwordx4 v[36:39], v23, %[srdw], s57 offen sc1\n"
    "buffer_load_dwordx4 v[40:43], v23, %[srdw], s58 offen sc1\n"
    "buffer_load_dwordx4 v[44:47], v23, %[srdw], s59 offen sc1\n"
    "buffer_load_dwordx4 v[48:51], v23, %[srdw], s60 offen sc1\n"
    "buffer_load_dwordx4 v[52:55], v23, %[srdw], s61 offen sc1\n"
    "buffer_load_dwordx4 v[56:59], v23, %[srdw], s62 offen sc1\n"
    "v_cmp_eq_u64 vcc, v[98:99], v[26:27]\n"
    "s_nop 0\n"
    "s_or_b64 vcc, vcc, s[88:89]\n"
    "s_cmp_eq_u64 vcc, -1\n"
    "s_cbranch_scc1 Lb1%=\n"
    "Lq2%=:\n"
    "Lh13_%=:\n"
    "s_cbranch_execz Lr13_%=\n"
    "s_mov_b64 exec, 0xff\n"
    "s_mov_b32 s79, 0x100000\n"
    "Lsp%=:\n"
    "buffer_load_dwordx2 v[100:101], v22, %[srdw], %[cbase] offen offset:8 sc1\n"
    "s_waitcnt vmcnt(0)\n"
    "v_cmp_eq_u64 vcc, v[100:101], v[26:27]\n"
    "s_nop 0\n"
    "s_or_b64 vcc, vcc, s[88:89]\n"
    "s_cmp_eq_u64 vcc, -1\n"
    "s_cbranch_scc1 Lse%=\n"
    "s_sleep 1\n"
    "s_sub_u32 s79, s79, 1\n"
    "s_cbranch_scc0 Lsp%=\n"
    "Lse%=:\n"
    "s_mov_b64 exec, -1\n"
    "Lb1%=:\n"
    "buffer_load_dwordx2 v[160:161], v24, %[srdw], s56 offen offset:128 sc1\n"
    "buffer_load_dwordx2 v[162:163], v24, %[srdw], s57 offen offset:128 sc1\n"
    "buffer_load_dwordx2 v[164:165], v24, %[srdw], s58 offen offset:128 sc1\n"
    "buffer_load_dwordx2 v[166:167], v24, %[srdw], s59 offen offset:128 sc1\n"
    "buffer_load_dwordx2 v[168:169], v24, %[srdw], s60 offen offset:128 sc1\n"
    "buffer_load_dwordx2 v[170:171], v24, %[srdw], s61 offen offset:128 sc1\n"
    "buffer_load_dwordx2 v[172:173], v24, %[srdw], s62 offen offset:128 sc1\n"
    "v_add_u32 v216, 4096, v23\n"
    "buffer_load_dwordx4 v[128:131], v216, %[srdw], s56 offen sc1\n"
    "buffer_load_dwordx4 v[132:135], v216, %[srdw], s57 offen sc1\n"
    "buffer_load_dwordx4 v[136:139], v216, %[srdw], s58 offen sc1\n"
    "buffer_load_dwordx4 v[140:143], v216, %[srdw], s59 offen sc1\n"
    "buffer_load_dwordx4 v[144:147], v216, %[srdw], s60 offen sc1\n"
    "buffer_load_dwordx4 v[148:151], v216, %[srdw], s61 offen sc1\n"
    "buffer_load_dwordx4 v[152:155], v216, %[srdw], s62 offen sc1\n"
    "s_waitcnt vmcnt(14)\n"
    "s_cmp_eq_u32 s80, 1\n"
    "s_cbranch_scc0 Lnc%=\n"
    "v_accvgpr_read_b32 v32, a214\n"
    "v_accvgpr_read_b32 v33, a215\n"
    "v_accvgpr_read_b32 v34, a216\n"
    "v_accvgpr_read_b32 v35, a217\n"
    "v_accvgpr_read_b32 v36, a218\n"
    "v_accvgpr_read_b32 v37, a219\n"
    "v_accvgpr_read_b32 v38, a220\n"
    "v_accvgpr_read_b32 v39, a221\n"
    "v_accvgpr_read_b32 v40, a222\n"
    "v_accvgpr_read_b32 v41, a223\n"
    "v_accvgpr_read_b32 v42, a224\n"
    "v_accvgpr_read_b32 v43, a225\n"
    "v_accvgpr_read_b32 v44, a226\n"
    "v_accvgpr_read_b32 v45, a227\n"
    "v_accvgpr_read_b32 v46, a228\n"
    "v_accvgpr_read_b32 v47, a229\n"
    "v_accvgpr_read_b32 v48, a230\n"
    "v_accvgpr_read_b32 v49, a231\n"
    "v_accvgpr_read_b32 v50, a232\n"
    "v_accvgpr_read_b32 v51, a233\n"
    "v_accvgpr_read_b32 v52, a234\n"
    "v_accvgpr_read_b32 v53, a235\n"
    "v_accvgpr_read_b32 v54, a236\n"
    "v_accvgpr_read_b32 v55, a237\n"
    "v_accvgpr_read_b32 v56, a238\n"
    "v_accvgpr_read_b32 v57, a239\n"
    "v_accvgpr_read_b32 v58, a240\n"
    "v_accvgpr_read_b32 v59, a241\n"
    "v_accvgpr_read_b32 v64, a242\n"
    "v_accvgpr_read_b32 v65, a243\n"
    "v_accvgpr_read_b32 v66, a244\n"
    "v_accvgpr_read_b32 v67, a245\n"
    "v_accvgpr_read_b32 v68, a246\n"
    "v_accvgpr_read_b32 v69, a247\n"
    "v_accvgpr_read_b32 v70, a248\n"
    "v_accvgpr_read_b32 v71, a249\n"
    "v_accvgpr_read_b32 v72, a250\n"
    "v_accvgpr_read_b32 v73, a251\n"
    "v_accvgpr_read_b32 v74, a252\n"
    "v_accvgpr_read_b32 v75, a253\n"
    "v_accvgpr_read_b32 v76, a254\n"
    "v_accvgpr_read_b32 v77, a255\n"
    "Lnc%=:\n"
    "v_max3_f32 v218, v64, v66, v68\n"
    "v_max3_f32 v219, v70, v72, v74\n"
    "v_max3_f32 v218, v218, v76, v78\n"
    "v_max_f32 v218, v218, v219\n"
    "v_sub_f32 v200, v64, v218\n"
    "v_sub_f32 v202, v66, v218\n"
    "v_sub_f32 v204, v68, v218\n"
    "v_sub_f32 v206, v70, v218\n"
    "v_sub_f32 v208, v72, v218\n"
    "v_sub_f32 v210, v74, v218\n"
    "v_sub_f32 v212, v76, v218\n"
    "v_sub_f32 v214, v78, v218\n"
    "v_exp_f32 v200, v200\n"
    "v_exp_f32 v202, v202\n"
    "v_exp_f32 v204, v204\n"
    "v_exp_f32 v206, v206\n"
    "v_exp_f32 v208, v208\n"
    "v_exp_f32 v210, v210\n"
    "v_exp_f32 v212, v212\n"
    "v_exp_f32 v214, v214\n"
    "v_mul_f32 v218, v200, v65\n"
    "v_mul_f32 v219, v202, v67\n"
    "v_fma_f32 v218, v204, v69, v218\n"
    "v_fma_f32 v219, v206, v71, v219\n"
    "v_fma_f32 v218, v208, v73, v218\n"
    "v_fma_f32 v219, v210, v75, v219\n"
    "v_fma_f32 v218, v212, v77, v218\n"
    "v_fma_f32 v219, v214, v79, v219\n"
    "v_add_f32 v218, v218, v219\n"
    "v_rcp_f32 v218, v218\n"
    "s_nop 0\n"
    "v_mul_f32 v200, v200, v218\n"
    "v_mul_f32 v202, v202, v218\n"
    "v_mul_f32 v204, v204, v218\n"
    "v_mul_f32 v206, v206, v218\n"
    "v_mul_f32 v208, v208, v218\n"
    "v_mul_f32 v210, v210, v218\n"
    "v_mul_f32 v212, v212, v218\n"
    "v_mul_f32 v214, v214, v218\n"
    "v_pk_mul_f32 v[192:193], v[176:177], v[214:215] op_sel_hi:[1,0]\n"
    "v_pk_mul_f32 v[194:195], v[178:179], v[214:215] op_sel_hi:[1,0]\n"
    "v_pk_mul_f32 v[196:197], v[180:181], v[214:215] op_sel_hi:[1,0]\n"
    "v_pk_mul_f32 v[198:199], v[182:183], v[214:215] op_sel_hi:[1,0]\n"
    "v_lshlrev_b32 v232, 16, v32\n"
    "v_and_b32 v233, 0xffff0000, v32\n"
    "v_lshlrev_b32 v234, 16, v33\n"
    "v_and_b32 v235, 0xffff0000, v33\n"
    "v_lshlrev_b32 v236, 16, v34\n"
    "v_and_b32 v237, 0xffff0000, v34\n"
    "v_lshlrev_b32 v238, 16, v35\n"
    "v_and_b32 v239, 0xffff0000, v35\n"
    "v_pk_fma_f32 v[192:193], v[232:233], v[200:201], v[192:193] op_sel_hi:[1,0,1]\n"
    "v_pk_fma_f32 v[194:195], v[234:235], v[200:201], v[194:195] op_sel_hi:[1,0,1]\n"
    "v_pk_fma_f32 v[196:197], v[236:237], v[200:201], v[196:197] op_sel_hi:[1,0,1]\n"
    "v_pk_fma_f32 v[198:199], v[238:239], v[200:201], v[198:199] op_sel_hi:[1,0,1]\n"
    "v_lshlrev_b32 v240, 16, v36\n"
    "v_and_b32 v241, 0xffff0000, v36\n"
    "v_lshlrev_b32 v242, 16, v37\n"
    "v_and_b32 v243, 0xffff0000, v37\n"
    "v_lshlrev_b32 v244, 16, v38\n"
    "v_and_b32 v245, 0xffff0000, v38\n"
    "v_lshlrev_b32 v246, 16, v39\n"
    "v_and_b32 v247, 0xffff0000, v39\n"
    "v_pk_fma_f32 v[192:193], v[240:241], v[202:203], v[192:193] op_sel_hi:[1,0,1]\n"
    "v_pk_fma_f32 v[194:195], v[242:243], v[202:203], v[194:195] op_sel_hi:[1,0,1]\n"
    "v_pk_fma_f32 v[196:197], v[244:245], v[202:203], v[196:197] op_sel_hi:[1,0,1]\n"
    "v_pk_fma_f32 v[198:199], v[246:247], v[202:203], v[198:199] op_sel_hi:[1,0,1]\n"
    "v_lshlrev_b32 v232, 16, v40\n"
    "v_and_b32 v233, 0xffff0000, v40\n"
    "v_lshlrev_b32 v234, 16, v41\n"
    "v_and_b32 v235, 0xffff0000, v41\n"
    "v_lshlrev_b32 v236, 16, v42\n"
    "v_and_b32 v237, 0xffff0000, v42\n"
    "v_lshlrev_b32 v238, 16, v43\n"
    "v_and_b32 v239, 0xffff0000, v43\n"
    "v_pk_fma_f32 v[192:193], v[232:233], v[204:205], v[192:193] op_sel_hi:[1,0,1]\n"
    "v_pk_fma_f32 v[194:195], v[234:235], v[204:205], v[194:195] op_sel_hi:[1,0,1]\n"
    "v_pk_fma_f32 v[196:197], v[236:237], v[204:205], v[196:197] op_sel_hi:[1,0,1]\n"
    "v_pk_fma_f32 v[198:199], v[238:239], v[204:205], v[198:199] op_sel_hi:[1,0,1]\n"
    "v_lshlrev_b32 v240, 16, v44\n"
    "v_and_b32 v241, 0xffff0000, v44\n"
    "v_lshlrev_b32 v242, 16, v45\n"
    "v_and_b32 v243, 0xffff0000, v45\n"
    "v_lshlrev_b32 v244, 16, v46\n"
    "v_and_b32 v245, 0xffff0000, v46\n"
    "v_lshlrev_b32 v246, 16, v47\n"
    "v_and_b32 v247, 0xffff0000, v47\n"
    "v_pk_fma_f32 v[192:193], v[240:241], v[206:207], v[192:193] op_sel_hi:[1,0,1]\n"
    "v_pk_fma_f32 v[194:195], v[242:243], v[206:207], v[194:195] op_sel_hi:[1,0,1]\n"
    "v_pk_fma_f32 v[196:197], v[244:245], v[206:207], v[196:197] op_sel_hi:[1,0,1]\n"
    "v_pk_fma_f32 v[198:199], v[246:247], v[206:207], v[198:199] op_sel_hi:[1,0,1]\n"
    "v_lshlrev_b32 v232, 16, v48\n"
    "v_and_b32 v233, 0xffff0000, v48\n"
    "v_lshlrev_b32 v234, 16, v49\n"
    "v_and_b32 v235, 0xffff0000, v49\n"
    "v_lshlrev_b32 v236, 16, v50\n"
    "v_and_b32 v237, 0xffff0000, v50\n"
    "v_lshlrev_b32 v238, 16, v51\n"
    "v_and_b32 v239, 0xffff0000, v51\n"
    "v_pk_fma_f32 v[192:193], v[232:233], v[208:209], v[192:193] op_sel_hi:[1,0,1]\n"
    "v_pk_fma_f32 v[194:195], v[234:235], v[208:209], v[194:195] op_sel_hi:[1,0,1]\n"
    "v_pk_fma_f32 v[196:197], v[236:237], v[208:209], v[196:197] op_sel_hi:[1,0,1]\n"
    "v_pk_fma_f32 v[198:199], v[238:239], v[208:209], v[198:199] op_sel_hi:[1,0,1]\n"
    "v_lshlrev_b32 v240, 16, v52\n"
    "v_and_b32 v241, 0xffff0000, v52\n"
    "v_lshlrev_b32 v242, 16, v53\n"
    "v_and_b32 v243, 0xffff0000, v53\n"
    "v_lshlrev_b32 v244, 16, v54\n"
    "v_and_b32 v245, 0xffff0000, v54\n"
    "v_lshlrev_b32 v246, 16, v55\n"
    "v_and_b32 v247, 0xffff0000, v55\n"
    "v_pk_fma_f32 v[192:193], v[240:241], v[210:211], v[192:193] op_sel_hi:[1,0,1]\n"
    "v_pk_fma_f32 v[194:195], v[242:243], v[210:211], v[194:195] op_sel_hi:[1,0,1]\n"
    "v_pk_fma_f32 v[196:197], v[244:245], v[210:211], v[196:197] op_sel_hi:[1,0,1]\n"
    "v_pk_fma_f32 v[198:199], v[246:247], v[210:211], v[198:199] op_sel_hi:[1,0,1]\n"
    "v_lshlrev_b32 v232, 16, v56\n"
    "v_and_b32 v233, 0xffff0000, v56\n"
    "v_lshlrev_b32 v234, 16, v57\n"
    "v_and_b32 v235, 0xffff0000, v57\n"
    "v_lshlrev_b32 v236, 16, v58\n"
    "v_and_b32 v237, 0xffff0000, v58\n"
    "v_lshlrev_b32 v238, 16, v59\n"
    "v_and_b32 v239, 0xffff0000, v59\n"
    "v_pk_fma_f32 v[192:193], v[232:233], v[212:213], v[192:193] op_sel_hi:[1,0,1]\n"
    "v_pk_fma_f32 v[194:195], v[234:235], v[212:213], v[194:195] op_sel_hi:[1,0,1]\n"
    "v_pk_fma_f32 v[196:197], v[236:237], v[212:213], v[196:197] op_sel_hi:[1,0,1]\n"
    "v_pk_fma_f32 v[198:199], v[238:239], v[212:213], v[198:199] op_sel_hi:[1,0,1]\n"
    "v_cvt_pk_bf16_f32 v224, v192, v193\n"
    "v_cvt_pk_bf16_f32 v225, v194, v195\n"
    "v_cvt_pk_bf16_f32 v226, v196, v197\n"
    "v_cvt_pk_bf16_f32 v227, v198, v199\n"
    "s_movk_i32 s79, 0x1000\n"
    "buffer_store_dwordx2 v[224:225], v25, %[srdo], 0 offen\n"
    "buffer_store_dwordx2 v[226:227], v25, %[srdo], 0 offen offset:32\n"
    "s_mov_b64 exec, 1\n"
    "buffer_store_dwordx2 v[26:27], off, %[srdw], %[epw] sc1\n"
    "s_mov_b64 exec, -1\n"
    "s_waitcnt vmcnt(10)\n"
    "v_max3_f32 v218, v160, v162, v164\n"
    "v_max3_f32 v219, v166, v168, v170\n"
    "v_max3_f32 v218, v218, v172, v174\n"
    "v_max_f32 v218, v218, v219\n"
    "v_sub_f32 v200, v160, v218\n"
    "v_sub_f32 v202, v162, v218\n"
    "v_sub_f32 v204, v164, v218\n"
    "v_sub_f32 v206, v166, v218\n"
    "v_sub_f32 v208, v168, v218\n"
    "v_sub_f32 v210, v170, v218\n"
    "v_sub_f32 v212, v172, v218\n"
    "v_sub_f32 v214, v174, v218\n"
    "v_exp_f32 v200, v200\n"
    "v_exp_f32 v202, v202\n"
    "v_exp_f32 v204, v204\n"
    "v_exp_f32 v206, v206\n"
    "v_exp_f32 v208, v208\n"
    "v_exp_f32 v210, v210\n"
    "v_exp_f32 v212, v212\n"
    "v_exp_f32 v214, v214\n"
    "v_mul_f32 v218, v200, v161\n"
    "v_mul_f32 v219, v202, v163\n"
    "v_fma_f32 v218, v204, v165, v218\n"
    "v_fma_f32 v219, v206, v167, v219\n"
    "v_fma_f32 v218, v208, v169, v218\n"
    "v_fma_f32 v219, v210, v171, v219\n"
    "v_fma_f32 v218, v212, v173, v218\n"
    "v_fma_f32 v219, v214, v175, v219\n"
    "v_add_f32 v218, v218, v219\n"
    "v_rcp_f32 v218, v218\n"
    "s_nop 0\n"
    "v_mul_f32 v200, v200, v218\n"
    "v_mul_f32 v202, v202, v218\n"
    "v_mul_f32 v204, v204, v218\n"
    "v_mul_f32 v206, v206, v218\n"
    "v_mul_f32 v208, v208, v218\n"
    "v_mul_f32 v210, v210, v218\n"
    "v_mul_f32 v212, v212, v218\n"
    "v_mul_f32 v214, v214, v218\n"
    "v_pk_mul_f32 v[192:193], v[184:185], v[214:215] op_sel_hi:[1,0]\n"
    "v_pk_mul_f32 v[194:195], v[186:187], v[214:215] op_sel_hi:[1,0]\n"
    "v_pk_mul_f32 v[196:197], v[188:189], v[214:215] op_sel_hi:[1,0]\n"
    "v_pk_mul_f32 v[198:199], v[190:191], v[214:215] op_sel_hi:[1,0]\n"
    "s_waitcnt vmcnt(9)\n"
    "v_lshlrev_b32 v232, 16, v128\n"
    "v_and_b32 v233, 0xffff0000, v128\n"
    "v_lshlrev_b32 v234, 16, v129\n"
    "v_and_b32 v235, 0xffff0000, v129\n"
    "v_lshlrev_b32 v236, 16, v130\n"
    "v_and_b32 v237, 0xffff0000, v130\n"
    "v_lshlrev_b32 v238, 16, v131\n"
    "v_and_b32 v239, 0xffff0000, v131\n"
    "v_pk_fma_f32 v[192:193], v[232:233], v[200:201], v[192:193] op_sel_hi:[1,0,1]\n"
    "v_pk_fma_f32 v[194:195], v[234:235], v[200:201], v[194:195] op_sel_hi:[1,0,1]\n"
    "v_pk_fma_f32 v[196:197], v[236:237], v[200:201], v[196:197] op_sel_hi:[1,0,1]\n"
    "v_pk_fma_f32 v[198:199], v[238:239], v[200:201], v[198:199] op_sel_hi:[1,0,1]\n"
    "s_waitcnt vmcnt(8)\n"
    "v_lshlrev_b32 v240, 16, v132\n"
    "v_and_b32 v241, 0xffff0000, v132\n"
    "v_lshlrev_b32 v242, 16, v133\n"
    "v_and_b32 v243, 0xffff0000, v133\n"
    "v_lshlrev_b32 v244, 16, v134\n"
    "v_and_b32 v245, 0xffff0000, v134\n"
    "v_lshlrev_b32 v246, 16, v135\n"
    "v_and_b32 v247, 0xffff0000, v135\n"
    "v_pk_fma_f32 v[192:193], v[240:241], v[202:203], v[192:193] op_sel_hi:[1,0,1]\n"
    "v_pk_fma_f32 v[194:195], v[242:243], v[202:203], v[194:195] op_sel_hi:[1,0,1]\n"
    "v_pk_fma_f32 v[196:197], v[244:245], v[202:203], v[196:197] op_sel_hi:[1,0,1]\n"
    "v_pk_fma_f32 v[198:199], v[246:247], v[202:203], v[198:199] op_sel_hi:[1,0,1]\n"
    "s_waitcnt vmcnt(7)\n"
    "v_lshlrev_b32 v232, 16, v136\n"
    "v_and_b32 v233, 0xffff0000, v136\n"
    "v_lshlrev_b32 v234, 16, v137\n"
    "v_and_b32 v235, 0xffff0000, v137\n"
    "v_lshlrev_b32 v236, 16, v138\n"
    "v_and_b32 v237, 0xffff0000, v138\n"
    "v_lshlrev_b32 v238, 16, v139\n"
    "v_and_b32 v239, 0xffff0000, v139\n"
    "v_pk_fma_f32 v[192:193], v[232:233], v[204:205], v[192:193] op_sel_hi:[1,0,1]\n"
    "v_pk_fma_f32 v[194:195], v[234:235], v[204:205], v[194:195] op_sel_hi:[1,0,1]\n"
    "v_pk_fma_f32 v[196:197], v[236:237], v[204:205], v[196:197] op_sel_hi:[1,0,1]\n"
    "v_pk_fma_f32 v[198:199], v[238:239], v[204:205], v[198:199] op_sel_hi:[1,0,1]\n"
    "s_waitcnt vmcnt(6)\n"
    "v_lshlrev_b32 v240, 16, v140\n"
    "v_and_b32 v241, 0xffff0000, v140\n"
    "v_lshlrev_b32 v242, 16, v141\n"
    "v_and_b32 v243, 0xffff0000, v141\n"
    "v_lshlrev_b32 v244, 16, v142\n"
    "v_and_b32 v245, 0xffff0000, v142\n"
    "v_lshlrev_b32 v246, 16, v143\n"
    "v_and_b32 v247, 0xffff0000, v143\n"
    "v_pk_fma_f32 v[192:193], v[240:241], v[206:207], v[192:193] op_sel_hi:[1,0,1]\n"
    "v_pk_fma_f32 v[194:195], v[242:243], v[206:207], v[194:195] op_sel_hi:[1,0,1]\n"
    "v_pk_fma_f32 v[196:197], v[244:245], v[206:207], v[196:197] op_sel_hi:[1,0,1]\n"
    "v_pk_fma_f32 v[198:199], v[246:247], v[206:207], v[198:199] op_sel_hi:[1,0,1]\n"
    "s_waitcnt vmcnt(5)\n"
    "v_lshlrev_b32 v232, 16, v144\n"
    "v_and_b32 v233, 0xffff0000, v144\n"
    "v_lshlrev_b32 v234, 16, v145\n"
    "v_and_b32 v235, 0xffff0000, v145\n"
    "v_lshlrev_b32 v236, 16, v146\n"
    "v_and_b32 v237, 0xffff0000, v146\n"
    "v_lshlrev_b32 v238, 16, v147\n"
    "v_and_b32 v239, 0xffff0000, v147\n"
    "v_pk_fma_f32 v[192:193], v[232:233], v[208:209], v[192:193] op_sel_hi:[1,0,1]\n"
    "v_pk_fma_f32 v[194:195], v[234:235], v[208:209], v[194:195] op_sel_hi:[1,0,1]\n"
    "v_pk_fma_f32 v[196:197], v[236:237], v[208:209], v[196:197] op_sel_hi:[1,0,1]\n"
    "v_pk_fma_f32 v[198:199], v[238:239], v[208:209], v[198:199] op_sel_hi:[1,0,1]\n"
    "s_waitcnt vmcnt(4)\n"
    "v_lshlrev_b32 v240, 16, v148\n"
    "v_and_b32 v241, 0xffff0000, v148\n"
    "v_lshlrev_b32 v242, 16, v149\n"
    "v_and_b32 v243, 0xffff0000, v149\n"
    "v_lshlrev_b32 v244, 16, v150\n"
    "v_and_b32 v245, 0xffff0000, v150\n"
    "v_lshlrev_b32 v246, 16, v151\n"
    "v_and_b32 v247, 0xffff0000, v151\n"
    "v_pk_fma_f32 v[192:193], v[240:241], v[210:211], v[192:193] op_sel_hi:[1,0,1]\n"
    "v_pk_fma_f32 v[194:195], v[242:243], v[210:211], v[194:195] op_sel_hi:[1,0,1]\n"
    "v_pk_fma_f32 v[196:197], v[244:245], v[210:211], v[196:197] op_sel_hi:[1,0,1]\n"
    "v_pk_fma_f32 v[198:199], v[246:247], v[210:211], v[198:199] op_sel_hi:[1,0,1]\n"
    "s_waitcnt vmcnt(3)\n"
    "v_lshlrev_b32 v232, 16, v152\n"
    "v_and_b32 v233, 0xffff0000, v152\n"
    "v_lshlrev_b32 v234, 16, v153\n"
    "v_and_b32 v235, 0xffff0000, v153\n"
    "v_lshlrev_b32 v236, 16, v154\n"
    "v_and_b32 v237, 0xffff0000, v154\n"
    "v_lshlrev_b32 v238, 16, v155\n"
    "v_and_b32 v239, 0xffff0000, v155\n"
    "v_pk_fma_f32 v[192:193], v[232:233], v[212:213], v[192:193] op_sel_hi:[1,0,1]\n"
    "v_pk_fma_f32 v[194:195], v[234:235], v[212:213], v[194:195] op_sel_hi:[1,0,1]\n"
    "v_pk_fma_f32 v[196:197], v[236:237], v[212:213], v[196:197] op_sel_hi:[1,0,1]\n"
    "v_pk_fma_f32 v[198:199], v[238:239], v[212:213], v[198:199] op_sel_hi:[1,0,1]\n"
    "v_cvt_pk_bf16_f32 v228, v192, v193\n"
    "v_cvt_pk_bf16_f32 v229, v194, v195\n"
    "v_cvt_pk_bf16_f32 v230, v196, v197\n"
    "v_cvt_pk_bf16_f32 v231, v198, v199\n"
    "buffer_store_dwordx2 v[228:229], v25, %[srdo], s79 offen\n"
    "buffer_store_dwordx2 v[230:231], v25, %[srdo], s79 offen offset:32\n"
    "s_endpgm\n"
    "Lredo%=:\n"
    "s_waitcnt vmcnt(0)\n"
    "Lend%=:\n"
    "s_nop 1\n"
      : [m0] "+v"(m0), [m1] "+v"(m1), [m2] "+v"(m2), [m3] "+v"(m3), [taglo] "+v"(taglo), [taghi] "+v"(taghi), [bad] "=&s"(bad), [redo] "=&s"(redo)
      : [kbase] "v"(kbase), [vbase] "v"(vbase), [voffk] "v"(voffk), [voffv] "v"(voffv), [voffq] "v"(voffq),
        [srdk] "s"(srdk), [srdv] "s"(srdv), [srdq] "s"(srdq), [srdw] "s"(srdw), [srdkt] "s"(srdkt), [srdvt] "s"(srdvt), [srdm] "s"(srdm),
        [m0w] "s"(m0w), [mode] "s"(modes), [pb0] "s"(pb0), [pb1] "s"(pb1), [own] "s"(own), [sbase] "s"(sbase), [epo] "s"(epo), [cbase] "s"(cbase), [epw] "s"(epw), [sp] "s"(s), [srdo] "s"(srdo)
      : "memory", "scc", "vcc", "s56","s57","s58","s59","s60","s61","s62","s78","s79","s80","s81","s82","s83","s86","s87","s88","s89","s90","s91","s94","s95", "v18","v19","v20","v21","v22","v23","v24","v25","v26","v27","v28","v29","v30","v31","v32","v33","v34","v35","v36","v37","v38","v39","v40","v41","v42","v43","v44","v45","v46","v47","v48","v49","v50","v51","v52","v53","v54","v55","v56","v57","v58","v59","v60","v61","v62","v63","v64","v65","v66","v67","v68","v69","v70","v71","v72","v73","v74","v75","v76","v77","v78","v79","v80","v81","v82","v83","v84","v85","v86","v87","v88","v89","v90","v91","v92","v93","v94","v95","v96","v97","v98","v99","v100","v101","v102","v103","v104","v105","v106","v107","v108","v109","v110","v111","v112","v113","v114","v115","v116","v117","v118","v119","v120","v121","v122","v123","v124","v125","v126","v127","v128","v129","v130","v131","v132","v133","v134","v135","v136","v137","v138","v139","v140","v141","v142","v143","v144","v145","v146","v147","v148","v149","v150","v151","v152","v153","v154","v155","v156","v157","v158","v159","v160","v161","v162","v163","v164","v165","v166","v167","v168","v169","v170","v171","v172","v173","v174","v175","v176","v177","v178","v179","v180","v181","v182","v183","v184","v185","v186","v187","v188","v189","v190","v191","v192","v193","v194","v195","v196","v197","v198","v199","v200","v201","v202","v203","v204","v205","v206","v207","v208","v209","v210","v211","v212","v213","v214","v215","v216","v217","v218","v219","v220","v221","v222","v223","v224","v225","v226","v227","v228","v229","v230","v231","v232","v233","v234","v235","v236","v237","v238","v239","v240","v241","v242","v243","v244","v245","v246","v247","v248","v249","v250","v251","v252","v253","v254","v255", "a0","a1","a2","a3","a4","a5","a6","a7","a8","a9","a10","a11","a12","a13","a14","a15","a16","a17","a18","a19","a20","a21","a22","a23","a24","a25","a26","a27","a28","a29","a30","a31","a32","a33","a34","a35","a36","a37","a38","a39","a40","a41","a42","a43","a44","a45","a46","a47","a48","a49","a50","a51","a52","a53","a54","a55","a56","a57","a58","a59","a60","a61","a62","a63","a64","a65","a66","a67","a68","a69","a70","a71","a72","a73","a74","a75","a76","a77","a78","a79","a80","a81","a82","a83","a84","a85","a86","a87","a88","a89","a90","a91","a92","a93","a94","a95","a96","a97","a98","a99","a100","a101","a102","a103","a104","a105","a106","a107","a108","a109","a110","a111","a112","a113","a114","a115","a116","a117","a118","a119","a120","a121","a122","a123","a124","a125","a126","a127","a128","a129","a130","a131","a132","a133","a134","a135","a136","a137","a138","a139","a140","a141","a142","a143","a144","a145","a146","a147","a148","a149","a150","a151","a152","a153","a154","a155","a156","a157","a158","a159","a160","a161","a162","a163","a164","a165","a166","a167","a168","a169","a170","a171","a172","a173","a174","a175","a176","a177","a178","a179","a180","a181","a182","a183","a184","a185","a186","a187","a188","a189","a190","a191","a192","a193","a194","a195","a196","a197","a198","a199","a200","a201","a202","a203","a204","a205","a206","a207","a208","a209","a210","a211","a212","a213","a214","a215","a216","a217","a218","a219","a220","a221","a222","a223","a224","a225","a226","a227","a228","a229","a230","a231","a232","a233","a234","a235","a236","a237","a238","a239","a240","a241","a242","a243","a244","a245","a246","a247","a248","a249","a250","a251","a252","a253","a254","a255");
    if (__builtin_expect(__builtin_amdgcn_readfirstlane(redo) == 0u, 1)) break;
    bad = __builtin_amdgcn_readfirstlane(bad);
    asm volatile("s_waitcnt vmcnt(0)\n\tv_mbcnt_lo_u32_b32 %0, -1, 0\n\tv_mbcnt_hi_u32_b32 %0, -1, %0" : "=v"(lane2) :: "memory");
    const unsigned lane = lane2, c = lane & 15u, lg = lane >> 4;
    {
      u32x4 qf[4][4];
      const char* qp = Q + ((size_t)g * 256u + 64u * w + c) * 256u + 16u * lg;
#pragma unroll
      for (int a = 0; a < 4; ++a)
#pragma unroll
        for (int d = 0; d < 4; ++d) qf[a][d] = *(gu32x4*)(qp + 4096 * a + 64 * d);
      float mx[4] = {-3.0e38f, -3.0e38f, -3.0e38f, -3.0e38f};
      const char* kp = K + kvoff + c * 256u + 16u * lg;
#pragma unroll 1
      for (int pass = 0; pass < 2; ++pass) {
        const float f = pass ? 1.0f : QSCALE;
#pragma unroll 1
        for (int u = 0; u < (pass ? 64 : 4); ++u) {
          u32x4 kf[4];
#pragma unroll
          for (int d = 0; d < 4; ++d) kf[d] = *(gu32x4*)(kp + (size_t)(u >> 1) * US + (u & 1) * 4096u + 64 * d);
#pragma unroll
          for (int a = 0; a < 4; ++a) { f32x4 sc = {};
#pragma unroll
            for (int d = 0; d < 4; ++d) sc = mfma16(kf[d], qf[a][d], sc);
            mx[a] = fmaxf(mx[a], f * fmaxf(fmaxf(sc[0], sc[1]), fmaxf(sc[2], sc[3]))); }
        }
        if (pass == 0) {
#pragma unroll
          for (int a = 0; a < 4; ++a)
#pragma unroll
            for (int d = 0; d < 4; ++d) qf[a][d] = scaleq(qf[a][d]);
        }
      }
      if (own) {
#pragma unroll
        for (int kb = 0; kb < 2; ++kb) {
          u32x4 kf[4];
#pragma unroll
          for (int d = 0; d < 4; ++d) kf[d] = *(gu32x4*)(Kt + ((size_t)g * 32u + 16u * kb + c) * 256u + 16u * lg + 64 * d);
#pragma unroll
          for (int a = 0; a < 4; ++a) { f32x4 sc = {};
#pragma unroll
            for (int d = 0; d < 4; ++d) sc = mfma16(kf[d], qf[a][d], sc);
            const u32x2 mw = *(gu32x2*)(Mk + ((size_t)(g >> 2) * 32u + 16u * (a & 1) + c) * 64u + 32u * kb + 8u * lg);
            if ((unsigned)(a >> 1) + 1u == own) {
#pragma unroll
              for (int r = 0; r < 4; ++r) mx[a] = fmaxf(mx[a], sc[r] + (1.0f - ((r & 1) ? bfhi(mw[r >> 1]) : bflo(mw[r >> 1]))) * (-1000000000.0f * LOG2E)); } }
        }
      }
      if (bad & 1u) { m0 = max4(mx[0]); m2 = max4(mx[2]); }
      if (bad & 2u) { m1 = max4(mx[1]); m3 = max4(mx[3]); }
      mode = 1;
      asm volatile("s_waitcnt vmcnt(0) lgkmcnt(0)" ::: "memory");
      __builtin_amdgcn_s_barrier();
    }
  }
  asm volatile(".Ltree_attn_end:");
}

extern "C" void kernel_launch(void* const* d_in, const int* in_sizes, int n_in, void* d_out, int out_size, void* d_ws, size_t ws_size, hipStream_t stream) {
  char* ws = (char*)d_ws;
  tree_attn_kernel<<<dim3(256), dim3(256), 0, stream>>>((const char*)d_in[0], (const char*)d_in[1], (const char*)d_in[2], (const char*)d_in[3], (const char*)d_in[4],
                                                       (const char*)d_in[5], (char*)d_out, ws);
}
```
